```python
import torch

def _softmax(x, axis=-1):
    return torch.softmax(x, dim=axis)
def _gelu(x):
    return torch.nn.functional.gelu(x, approximate="tanh")
def _silu(x):
    return torch.nn.functional.silu(x)
def _sigmoid(x):
    return torch.sigmoid(x)
def _layernorm(x, axis=-1, eps=1e-5):
    m = x.mean(dim=axis, keepdim=True)
    v = x.var(dim=axis, keepdim=True, unbiased=False)
    return (x - m) / torch.sqrt(v + eps)
def _scatter_add(tgt, idx, src, axis=0):
    return tgt.clone().index_add_(axis, idx, src)
def _segment_sum(src, seg, n):
    out = torch.zeros((n,) + src.shape[1:], dtype=src.dtype)
    return out.index_add_(0, seg, src)
def _pad(x, pw):
    flat = tuple(v for pair in reversed(pw) for v in pair)
    return torch.nn.functional.pad(x, flat)

kernel_name = 'sequential_preLN__ssm_conv_s4__dense_activation_relu__prompt'

def setup_inputs():

    _w = (lambda a: (lambda t: t.to(torch.bfloat16) if t.is_floating_point() else t)(torch.as_tensor(a)))
    def _A_setup():
        B, L, D, N = 1, 2048, 512, 128
        A_re = -(lambda _x: torch.exp(torch.as_tensor(_x)))((lambda *s: torch.randn(*s, dtype=torch.bfloat16) * 6.4)(D, N))
        A_im = torch.pi * (lambda *s: torch.randn(*s, dtype=torch.bfloat16) * 0.05)(D, N)
        B_re = (lambda *s: torch.randn(*s, dtype=torch.bfloat16) * 0.05)(D, N)
        B_im = (lambda *s: torch.randn(*s, dtype=torch.bfloat16) * 0.05)(D, N)
        C_re = (lambda *s: torch.randn(*s, dtype=torch.bfloat16) * 0.05)(D, N)
        C_im = (lambda *s: torch.randn(*s, dtype=torch.bfloat16) * 0.05)(D, N)
        log_dt = (lambda *s: torch.randn(*s, dtype=torch.bfloat16) * 0.05)(D) * 0.5 - 4.0
        Dskip = (lambda *s: torch.randn(*s, dtype=torch.bfloat16) * 0.05)(D)
        return {
            "u": _w((lambda *s: torch.randn(*s, dtype=torch.bfloat16) * 0.05)(B, L, D)),
            "A_re": _w(A_re),
            "A_im": _w(A_im),
            "B_re": _w(B_re),
            "B_im": _w(B_im),
            "C_re": _w(C_re),
            "C_im": _w(C_im),
            "log_dt": _w(log_dt),
            "Dskip": _w(Dskip),
        }
    _A_in = _A_setup()

    def _B_setup():
        B, S, D, Dff = 1, 2048, 512, 1792
        return {
            "x": _w((lambda *s: torch.randn(*s, dtype=torch.bfloat16) * 0.05)(B, S, D)),
            "w1": _w((lambda *s: torch.randn(*s, dtype=torch.bfloat16) * 0.05)(D, Dff)),
            "w2": _w((lambda *s: torch.randn(*s, dtype=torch.bfloat16) * 0.05)(Dff, D)),
        }
    _B_in = _B_setup()

    return {
        "A_u": _A_in["u"],
        "A_A_re": _A_in["A_re"],
        "A_A_im": _A_in["A_im"],
        "A_B_re": _A_in["B_re"],
        "A_B_im": _A_in["B_im"],
        "A_C_re": _A_in["C_re"],
        "A_C_im": _A_in["C_im"],
        "A_log_dt": _A_in["log_dt"],
        "A_Dskip": _A_in["Dskip"],
        "B_x": _B_in["x"],
        "B_w1": _B_in["w1"],
        "B_w2": _B_in["w2"],
    }

def reference(A_u, A_A_re, A_A_im, A_B_re, A_B_im, A_C_re, A_C_im, A_log_dt, A_Dskip, B_x, B_w1, B_w2):
    def _A_ref(u, A_re, A_im, B_re, B_im, C_re, C_im, log_dt, Dskip):
        Bsz, L, D = u.shape
        dt = torch.exp(log_dt)[:, None]
        A = A_re + 1j * A_im
        Bc = B_re + 1j * B_im
        Cc = C_re + 1j * C_im
        logdA = dt * A
        dB = dt * Bc
        l = (lambda x, dt: x.to(dt))(torch.arange(L), A_re.dtype)
        P = torch.exp(logdA[:, :, None] * l[None, None, :])
        K = torch.einsum("dn,dnl,dn->dl", Cc, P, dB).real
        L2 = 2 * L
        Kp = _pad(K, ((0, 0), (0, L)))
        up = (lambda x, *d: x.permute(*d))(u, 0, 2, 1)
        up = _pad(up, ((0, 0), (0, 0), (0, L)))
        Kf = (lambda x, axes=(-2,-1): torch.fft.rfft2(x.float(), dim=axes))(Kp[:, None, :], axes=(-2, -1))
        Uf = (lambda x, axes=(-2,-1): torch.fft.rfft2(x.float(), dim=axes))(up[:, :, None, :], axes=(-2, -1))
        Yf = Uf * Kf[None, :, :, :]
        y = (lambda x, s, axes=(-2,-1): torch.fft.irfft2(x, s=s, dim=axes).to(torch.bfloat16))(Yf, (1, L2), axes=(-2, -1))[:, :, 0, :L]
        y = (lambda x, *d: x.permute(*d))(y, 0, 2, 1) + u * Dskip[None, None, :]
        return y

    def _B_ref(x, w1, w2):
        h = torch.einsum("bsd,df->bsf", x, w1)
        zero = torch.zeros_like(h)
        h = torch.maximum(h, zero)
        return torch.einsum("bsf,fd->bsd", h, w2)

    x = A_u
    h = x + _A_ref(_layernorm(x, axis=-1), A_A_re, A_A_im, A_B_re, A_B_im, A_C_re, A_C_im, A_log_dt, A_Dskip)
    h2 = h + _B_ref(_layernorm(h, axis=-1), B_w1, B_w2)
    return h2
```

```cpp
#include <hip/hip_runtime.h>
#include <hip/hip_bf16.h>
#include <stdint.h>

typedef unsigned short u16;
static __device__ __forceinline__ float bf2f(u16 v){ return __uint_as_float(((unsigned)v)<<16); }
static __device__ __forceinline__ u16 f2bf(float f){ __hip_bfloat16 b = __float2bfloat16(f); return *reinterpret_cast<u16*>(&b); }
static __device__ __forceinline__ float rbf(float f){ return bf2f(f2bf(f)); }

constexpr int L=2048, D=512, NS=128, FF=1792;

__global__ void k_ln(const u16* __restrict__ x, u16* __restrict__ u){
  int row = blockIdx.x*4 + (threadIdx.x>>6); int lane = threadIdx.x&63;
  float v[8]; float s=0;
  for(int j=0;j<8;j++){ v[j]=bf2f(x[row*D+lane*8+j]); s+=v[j]; }
  for(int o=32;o>0;o>>=1) s+=__shfl_xor(s,o);
  float m=s*(1.f/D); float q=0;
  for(int j=0;j<8;j++){ float d=v[j]-m; q+=d*d; }
  for(int o=32;o>0;o>>=1) q+=__shfl_xor(q,o);
  float var=rbf(q*(1.f/D)); float mb=rbf(m);
  float sd=rbf(sqrtf(rbf(var+1e-5f)));
  for(int j=0;j<8;j++){ u[row*D+lane*8+j]=f2bf(rbf(v[j]-mb)/sd); }
}
__global__ void k_kgen(const u16* Are,const u16* Aim,const u16* Bre,const u16* Bim,const u16* Cre,const u16* Cim,const u16* logdt,float* K){
  int d=blockIdx.x; __shared__ float ar[NS],ai[NS],cr[NS],ci[NS];
  float dt=rbf(expf(bf2f(logdt[d])));
  if(threadIdx.x<NS){int n=threadIdx.x; ar[n]=dt*bf2f(Are[d*NS+n]); ai[n]=dt*bf2f(Aim[d*NS+n]);
    float br=dt*bf2f(Bre[d*NS+n]), bi=dt*bf2f(Bim[d*NS+n]); float c0=bf2f(Cre[d*NS+n]), c1=bf2f(Cim[d*NS+n]);
    cr[n]=c0*br-c1*bi; ci[n]=c0*bi+c1*br; }
  __syncthreads();
  for(int l=threadIdx.x;l<L;l+=blockDim.x){ float lf=rbf((float)l); float acc=0;
    for(int n=0;n<NS;n++){ float e=expf(ar[n]*lf); float sn,cs; sincosf(ai[n]*lf,&sn,&cs); acc+= e*(cr[n]*cs-ci[n]*sn);}
    K[d*L+l]=acc; }
}
__global__ void k_conv(const u16* x,const u16* u,const float* K,const u16* Dsk,u16* h){
  int d=blockIdx.x; __shared__ float us[L], ks[L];
  for(int l=threadIdx.x;l<L;l+=blockDim.x){ us[l]=bf2f(u[l*D+d]); ks[l]=K[d*L+l]; }
  __syncthreads();
  float dk=bf2f(Dsk[d]);
  for(int t=threadIdx.x;t<L;t+=blockDim.x){ float acc=0; for(int s=0;s<=t;s++) acc+=ks[t-s]*us[s];
    float y=rbf(acc); float z=rbf(y+rbf(us[t]*dk)); h[t*D+d]=f2bf(bf2f(x[t*D+d])+z); }
}
__global__ void k_g1(const u16* a,const u16* w1,u16* H){
  int t=blockIdx.y; int f=blockIdx.x*256+threadIdx.x; __shared__ float as[D];
  for(int i=threadIdx.x;i<D;i+=256) as[i]=bf2f(a[t*D+i]); __syncthreads();
  float acc=0; for(int k=0;k<D;k++) acc+=as[k]*bf2f(w1[k*FF+f]);
  H[t*FF+f]=f2bf(fmaxf(rbf(acc),0.f));
}
__global__ void k_g2(const u16* H,const u16* w2,const u16* h,u16* out){
  int t=blockIdx.y; int d=blockIdx.x*256+threadIdx.x; __shared__ float as[FF];
  for(int i=threadIdx.x;i<FF;i+=256) as[i]=bf2f(H[t*FF+i]); __syncthreads();
  float acc=0; for(int k=0;k<FF;k++) acc+=as[k]*bf2f(w2[k*D+d]);
  out[t*D+d]=f2bf(bf2f(h[t*D+d])+rbf(acc));
}
extern "C" void kernel_launch(void* const* d_in, const int* in_sizes, int n_in,
                              void* d_out, int out_size, void* d_ws, size_t ws_size,
                              hipStream_t stream) {
  const u16* x=(const u16*)d_in[0];
  char* ws=(char*)d_ws;
  u16* u=(u16*)ws; u16* h=(u16*)(ws+(2<<20)); u16* a2=(u16*)(ws+(4<<20)); float* K=(float*)(ws+(6<<20)); u16* H=(u16*)(ws+(10<<20));
  k_ln<<<L/4,256,0,stream>>>(x,u);
  k_kgen<<<D,256,0,stream>>>((const u16*)d_in[1],(const u16*)d_in[2],(const u16*)d_in[3],(const u16*)d_in[4],(const u16*)d_in[5],(const u16*)d_in[6],(const u16*)d_in[7],K);
  k_conv<<<D,256,0,stream>>>(x,u,K,(const u16*)d_in[8],h);
  k_ln<<<L/4,256,0,stream>>>(h,a2);
  k_g1<<<dim3(FF/256,L),256,0,stream>>>(a2,(const u16*)d_in[10],H);
  k_g2<<<dim3(D/256,L),256,0,stream>>>(H,(const u16*)d_in[11],h,(u16*)d_out);
}
```

```cpp
#include <hip/hip_runtime.h>
#include <stdint.h>

namespace s4k {
typedef unsigned short u16;
typedef unsigned long long u64;
typedef unsigned v4u __attribute__((ext_vector_type(4)));
typedef unsigned v2u __attribute__((ext_vector_type(2)));
typedef unsigned v8u __attribute__((ext_vector_type(8)));
typedef unsigned v16u __attribute__((ext_vector_type(16)));
typedef float f2 __attribute__((ext_vector_type(2)));
typedef _Float16 h2 __attribute__((ext_vector_type(2)));
typedef short s4 __attribute__((ext_vector_type(4)));
#define LAS __attribute__((address_space(3)))
#define GAS __attribute__((address_space(1)))
#define CAS __attribute__((address_space(4)))
#define RLX __ATOMIC_RELAXED, __HIP_MEMORY_SCOPE_AGENT
#define DI __device__ __forceinline__
template <class T, class U> DI T bit_as(const U x) { return __builtin_bit_cast(T, x); }
#define BC(T, x) bit_as<T>(x)
#define RFL(x) ((unsigned)__builtin_amdgcn_readfirstlane((int)(x)))

constexpr unsigned WS_E = 0;
constexpr unsigned WS_FA = 4096;
constexpr unsigned WS_FB = WS_FA + 16384;
constexpr unsigned WS_FC = WS_FB + 16384;
constexpr unsigned WS_UT = 1u << 20;
constexpr unsigned WS_SD = 3u << 20;
constexpr unsigned WS_HT = 5u << 20;
constexpr unsigned WS_HF = 7u << 20;
constexpr unsigned WS_PW = 15u << 20;
constexpr unsigned WS_PB = 17u << 20;
constexpr unsigned WS_END = 18u << 20;

constexpr unsigned L_RING = 0, L_W1B = 49152, L_W1A = 98304;
constexpr unsigned L_LN = 73728, LN_TS = 1024 + 64;
constexpr unsigned L_KC = 0, KC_CP = 4160, KC_CH = 2 * KC_CP;
constexpr unsigned L_UI = 2 * KC_CH, UI_H = 2304, UI_CH = 4672;
constexpr unsigned LDS_BYTES = 163840;

#define BAR() asm volatile("s_waitcnt lgkmcnt(0)\n\ts_barrier" ::: "memory")
#define VMWAIT(n) asm volatile("s_waitcnt vmcnt(" #n ")" ::: "memory")

template <int CTRL, int RM> DI float dpp_f(float v) { return BC(float, __builtin_amdgcn_update_dpp(0, BC(int, v), CTRL, RM, 0xf, RM == 0xf)); }
DI float wave_sum(float s) {
  s += dpp_f<0xB1, 0xf>(s); s += dpp_f<0x4E, 0xf>(s); s += dpp_f<0x141, 0xf>(s); s += dpp_f<0x140, 0xf>(s);
  s += dpp_f<0x142, 0xa>(s); s += dpp_f<0x143, 0xc>(s);
  return BC(float, __builtin_amdgcn_readlane(BC(int, s), 63));
}
DI float wave_max_nonneg(float s) {
  s = fmaxf(s, dpp_f<0xB1, 0xf>(s)); s = fmaxf(s, dpp_f<0x4E, 0xf>(s)); s = fmaxf(s, dpp_f<0x141, 0xf>(s)); s = fmaxf(s, dpp_f<0x140, 0xf>(s));
  s = fmaxf(s, dpp_f<0x142, 0xa>(s)); s = fmaxf(s, dpp_f<0x143, 0xc>(s));
  return BC(float, __builtin_amdgcn_readlane(BC(int, s), 63));
}
DI float rbf(float f) { return (float)(__bf16)f; }
DI float blo(unsigned v) { return BC(float, v << 16); }
DI float bhi(unsigned v) { return BC(float, v & 0xffff0000u); }
DI f2 bpair(unsigned v) { return f2{blo(v), bhi(v)}; }
DI unsigned pk_f16(float a, float b) { h2 p = {(_Float16)a, (_Float16)b}; return BC(unsigned, p); }
DI u64 mix64(u64 z) { z += 0x9E3779B97F4A7C15ull; z = (z ^ (z >> 30)) * 0xBF58476D1CE4E5B9ull; z = (z ^ (z >> 27)) * 0x94D049BB133111EBull; return z ^ (z >> 31); }
DI v2u tr8(LAS unsigned char* p) { return BC(v2u, __builtin_amdgcn_ds_read_tr16_b64_v4i16((LAS s4*)p)); }
DI v4u cat(v2u a, v2u b) { return v4u{a[0], a[1], b[0], b[1]}; }
DI float ln_std(float var) { return rbf(__builtin_amdgcn_sqrtf(rbf(rbf(fmaxf(var, 0.f)) + 1e-5f))); }
struct C2 { f2 re, im; };
DI C2 cmul(const C2& a, const C2& b) { return C2{a.re * b.re - a.im * b.im, a.re * b.im + a.im * b.re}; }
DI C2 cexpn(f2 ar, f2 ai, float n) {
  C2 r;
#pragma unroll
  for (int i = 0; i < 2; ++i) {
    const float e = __builtin_amdgcn_exp2f(ar[i] * n), a = __builtin_amdgcn_fractf(ai[i] * n);
    r.re[i] = e * __builtin_amdgcn_cosf(a); r.im[i] = e * __builtin_amdgcn_sinf(a);
  }
  return r;
}
template <int LO, int SO> DI void dma1(unsigned lds, unsigned voff, __amdgpu_buffer_rsrc_t rs, unsigned soff) {
  unsigned tmp;
  asm volatile("s_add_u32 m0, %1, %c5\n\ts_add_u32 %0, %4, %c6\n\tbuffer_load_dwordx4 %2, %3, %0 offen lds"
               : "=&s"(tmp) : "s"(lds), "v"(voff), "s"(rs), "s"(soff), "i"(LO), "i"(SO) : "memory", "scc");
}
DI void poll_sample(__amdgpu_buffer_rsrc_t rs, unsigned off) {
  asm volatile("buffer_load_dwordx4 v[240:243], %0, %1, 0 offen sc1\n\tbuffer_load_dwordx4 v[244:247], %0, %1, 0 offen offset:1024 sc1" :: "v"(off), "s"(rs) : "memory");
}
DI void poll_finish(__amdgpu_buffer_rsrc_t rs, unsigned off, unsigned lo, unsigned hi) {
  unsigned cnt;
  asm volatile("s_mov_b32 %0, 0\n"
               "PL%=:\n\ts_waitcnt vmcnt(0)\n\tv_xor_b32 v248, %3, v240\n\tv_bitop3_b32 v248, v241, %4, v248 bitop3:0xbe\n\tv_bitop3_b32 v248, v242, %3, v248 bitop3:0xbe\n\t"
               "v_bitop3_b32 v248, v243, %4, v248 bitop3:0xbe\n\tv_bitop3_b32 v248, v244, %3, v248 bitop3:0xbe\n\tv_bitop3_b32 v248, v245, %4, v248 bitop3:0xbe\n\t"
               "v_bitop3_b32 v248, v246, %3, v248 bitop3:0xbe\n\tv_bitop3_b32 v248, v247, %4, v248 bitop3:0xbe\n\tv_cmp_ne_u32 vcc, 0, v248\n\ts_nop 4\n\t"
               "s_cbranch_vccz PD%=\n\tbuffer_load_dwordx4 v[240:243], %1, %2, 0 offen sc1\n\tbuffer_load_dwordx4 v[244:247], %1, %2, 0 offen offset:1024 sc1\n\t"
               "s_add_u32 %0, %0, 1\n\ts_cmp_lt_u32 %0, 0x4000\n\ts_cbranch_scc1 PL%=\n\ts_waitcnt vmcnt(0)\n"
               "PD%=:"
               : "=&s"(cnt) : "v"(off), "s"(rs), "s"(lo), "s"(hi) : "memory", "scc", "vcc");
}

__global__ void __launch_bounds__(256, 1) __attribute__((amdgpu_num_vgpr(44)))
s4_mlp_kernel(const u16*, const u16*, const u16*, const u16*, const u16*, const u16*, const u16*, const u16*, const u16*, const u16*,
              const u16*, u16*, unsigned char*) {
  extern __shared__ __attribute__((aligned(16))) unsigned char smem[];
  LAS unsigned char* L = (LAS unsigned char*)smem;
  v16u ka; v8u kb_; v2u kw;
  asm volatile("s_load_dwordx16 %0, %3, 0x0\n\ts_load_dwordx8 %1, %3, 0x40\n\ts_load_dwordx2 %2, %3, 0x60\n\ts_waitcnt lgkmcnt(0)"
               : "=&s"(ka), "=&s"(kb_), "=&s"(kw) : "s"(__builtin_amdgcn_kernarg_segment_ptr()));
#define PTR(T, v, i) ((T)(((u64)(v)[2 * (i) + 1] << 32) | (v)[2 * (i)]))
  const GAS u16 *x = PTR(const GAS u16*, ka, 0), *Are = PTR(const GAS u16*, ka, 1), *Aim = PTR(const GAS u16*, ka, 2), *Bre = PTR(const GAS u16*, ka, 3),
                *Bim = PTR(const GAS u16*, ka, 4), *Cre = PTR(const GAS u16*, ka, 5), *Cim = PTR(const GAS u16*, ka, 6), *logdt = PTR(const GAS u16*, ka, 7),
                *Dsk = PTR(const GAS u16*, kb_, 0), *w1 = PTR(const GAS u16*, kb_, 1), *w2 = PTR(const GAS u16*, kb_, 2);
  GAS u16* out = PTR(GAS u16*, kb_, 3);
  GAS unsigned char* ws = PTR(GAS unsigned char*, kw, 0);
#undef PTR
  const int tid = threadIdx.x, lane = tid & 63, b = blockIdx.x;
  const unsigned w = RFL(tid >> 6);
  const int g4 = lane >> 4, r16 = lane & 15, q4 = r16 >> 2, p4 = lane & 3;
  const unsigned ch0 = (b & 7) * 64 + (b >> 3) * 2;
  const unsigned grp = (b & 7) * 2 + (b >> 7), mem = (b >> 3) & 15, ds = mem & 7, th = mem >> 3;
  const unsigned cw = w >> 1, sh = w & 1;

  v4u xv[2];
  unsigned pAre, pAim, pBre, pBim, pCre, pCim;
  asm volatile("global_load_dwordx4 %0, %8, %10\n\tglobal_load_dwordx4 %1, %8, %10 offset:1024\n\tglobal_load_dword %2, %9, %11\n\tglobal_load_dword %3, %9, %12\n\t"
               "global_load_dword %4, %9, %13\n\tglobal_load_dword %5, %9, %14\n\tglobal_load_dword %6, %9, %15\n\tglobal_load_dword %7, %9, %16\n\t"
               "v_mov_b32 v234, 0\n\tglobal_load_dwordx2 v[234:235], v234, %17 sc1"
               : "=&v"(xv[0]), "=&v"(xv[1]), "=&v"(pAre), "=&v"(pAim), "=&v"(pBre), "=&v"(pBim), "=&v"(pCre), "=&v"(pCim)
               : "v"((8 * b + 2 * w) * 1024 + lane * 16), "v"((ch0 + cw) * 256 + lane * 4), "s"(x), "s"(Are), "s"(Aim), "s"(Bre), "s"(Bim), "s"(Cre), "s"(Cim), "s"(ws)
               : "memory");
  const unsigned pdt = *(const CAS unsigned*)(u64)(logdt + ch0), pdk = *(const CAS unsigned*)(u64)(Dsk + ch0);
  asm volatile("v_mov_b32 v124, 0x3f803f80\n\tv_mov_b32 v125, 0x3f803f80\n\tv_mov_b32 v126, 0x3f803f80\n\tv_mov_b32 v127, 0x3f803f80" ::: "v255", "a255");

  const __amdgpu_buffer_rsrc_t rw1 = __builtin_amdgcn_make_buffer_rsrc((GAS void*)w1, 0, 512 * 1792 * 2, 0x00020000);
  const __amdgpu_buffer_rsrc_t rw2 = __builtin_amdgcn_make_buffer_rsrc((GAS void*)w2, 0, 512 * 1792 * 2, 0x00020000);
  const __amdgpu_buffer_rsrc_t rws = __builtin_amdgcn_make_buffer_rsrc((GAS void*)ws, 0, WS_END, 0x00020000);
  const unsigned ring = L_RING + w * 12288;
  const unsigned stA = L_W1A + w * 16384;
  const unsigned hf = WS_HF + (grp * 4 + w) * (112 * 1024) + lane * 16;
  const unsigned vA = (lane >> 3) * 3584 + (((lane & 7) ^ (((lane >> 4) & 3) << 1)) * 16);
  const unsigned sA = w * (128 * 3584) + mem * 128;
  const unsigned sW = WS_PW + (ds * 112 + 16 * w) * 2048, sV = WS_PW + (ds * 112 + 64 + 12 * w) * 2048, vW = cw * 1024 + lane * 16;
  const unsigned pm = 8 * (b & 1) + 2 * w + (lane >= 24), pl = lane >= 24 ? lane - 24 : lane;
  const unsigned pu = 4 * b + w;
  {
    const unsigned v2 = b < 224 ? (lane >> 3) * 1024 + (((lane & 7) ^ (((lane >> 4) & 1) << 2)) * 16) : 0x7ffffff0u;
    const unsigned so = (pu >> 3) * 16384 + (pu & 7) * 128;
    dma1<0, 0>(stA, v2, rw2, so);
    dma1<1024, 8192>(stA, v2, rw2, so);
    asm volatile("buffer_load_dwordx4 v[248:251], %0, %1, 0 offen"
                 :: "v"(lane < 48 ? (4 * (b >> 1) + (pl * 43 >> 8)) * 3584 + 2048 + pm * 96 + (pl - 6 * (pl * 43 >> 8)) * 16 : 0x7ffffff0u), "s"(rw1) : "memory");
    u64 pc;
    asm volatile("s_getpc_b64 %0" : "=s"(pc));
    const __amdgpu_buffer_rsrc_t rc = __builtin_amdgcn_make_buffer_rsrc((GAS void*)(pc & ~255ull), 0, 27648, 0x00020000);
    __builtin_amdgcn_s_sleep(6);
    asm volatile("buffer_load_dword v255, %0, %1, 0 offen" :: "v"(lane < 2 ? ((b >> 3) * 8 + 2 * w + lane) * 128 : 0x7ffffff0u), "s"(rc) : "memory");
    unsigned tmp;
    asm volatile(
      "s_add_u32 m0, %[mA], 12288\n\t"
      "s_add_u32 %[tmp], %[sA], 344064\n\t"
      "buffer_load_dwordx4 %[vA], %[rs], %[tmp] offen lds\n\t"
      "s_add_u32 m0, %[mA], 13312\n\t"
      "s_add_u32 %[tmp], %[sA], 372736\n\t"
      "buffer_load_dwordx4 %[vA], %[rs], %[tmp] offen lds\n\t"
      "s_add_u32 m0, %[mA], 14336\n\t"
      "s_add_u32 %[tmp], %[sA], 401408\n\t"
      "buffer_load_dwordx4 %[vA], %[rs], %[tmp] offen lds\n\t"
      "s_add_u32 m0, %[mA], 15360\n\t"
      "s_add_u32 %[tmp], %[sA], 430080\n\t"
      "buffer_load_dwordx4 %[vA], %[rs], %[tmp] offen lds\n\t"
        : [tmp] "=&s"(tmp) : [mA] "s"(stA), [sA] "s"(sA), [vA] "v"(vA), [rs] "s"(rw1) : "memory", "scc");
  }
  asm volatile(
      "v_bfe_i32 v48, %[oh], 0, 1\n\t"
      "v_and_b32 v51, 0x3f803f80, v48\n\t"
      "v_and_b32 v50, 0x3f803f80, v48\n\t"
      "v_and_b32 v49, 0x3f803f80, v48\n\t"
      "v_and_b32 v48, 0x3f803f80, v48\n\t"
      "v_bfe_i32 v52, %[oh], 1, 1\n\t"
      "v_and_b32 v55, 0x3f803f80, v52\n\t"
      "v_and_b32 v54, 0x3f803f80, v52\n\t"
      "v_and_b32 v53, 0x3f803f80, v52\n\t"
      "v_and_b32 v52, 0x3f803f80, v52\n\t"
      "v_bfe_i32 v56, %[oh], 2, 1\n\t"
      "v_and_b32 v59, 0x3f803f80, v56\n\t"
      "v_and_b32 v58, 0x3f803f80, v56\n\t"
      "v_and_b32 v57, 0x3f803f80, v56\n\t"
      "v_and_b32 v56, 0x3f803f80, v56\n\t"
      "v_bfe_i32 v60, %[oh], 3, 1\n\t"
      "v_and_b32 v63, 0x3f803f80, v60\n\t"
      "v_and_b32 v62, 0x3f803f80, v60\n\t"
      "v_and_b32 v61, 0x3f803f80, v60\n\t"
      "v_and_b32 v60, 0x3f803f80, v60\n\t"
      "v_bfe_i32 v64, %[oh], 4, 1\n\t"
      "v_and_b32 v67, 0x3f803f80, v64\n\t"
      "v_and_b32 v66, 0x3f803f80, v64\n\t"
      "v_and_b32 v65, 0x3f803f80, v64\n\t"
      "v_and_b32 v64, 0x3f803f80, v64\n\t"
      "v_bfe_i32 v68, %[oh], 5, 1\n\t"
      "v_and_b32 v71, 0x3f803f80, v68\n\t"
      "v_and_b32 v70, 0x3f803f80, v68\n\t"
      "v_and_b32 v69, 0x3f803f80, v68\n\t"
      "v_and_b32 v68, 0x3f803f80, v68\n\t"
      "v_bfe_i32 v72, %[oh], 6, 1\n\t"
      "v_and_b32 v75, 0x3f803f80, v72\n\t"
      "v_and_b32 v74, 0x3f803f80, v72\n\t"
      "v_and_b32 v73, 0x3f803f80, v72\n\t"
      "v_and_b32 v72, 0x3f803f80, v72\n\t"
      :: [oh] "v"(1u << r16));
  asm volatile("s_waitcnt vmcnt(15)" : "+v"(xv[0]), "+v"(xv[1]) :: "memory");

  v2u stat;
  {
    f2 v[2][4];
    float piv[2], sa[2], sq[2], sd[2], mb[2];
#pragma unroll
    for (int i = 0; i < 2; ++i) {
      piv[i] = BC(float, RFL(xv[i][0] << 16));
#pragma unroll
      for (int e = 0; e < 4; ++e) v[i][e] = bpair(xv[i][e]) - f2{piv[i], piv[i]};
      const f2 a = (v[i][0] + v[i][1]) + (v[i][2] + v[i][3]);
      const f2 q = (v[i][0] * v[i][0] + v[i][1] * v[i][1]) + (v[i][2] * v[i][2] + v[i][3] * v[i][3]);
      sa[i] = a[0] + a[1]; sq[i] = q[0] + q[1];
    }
#pragma unroll
    for (int i = 0; i < 2; ++i) {
      const float m0 = wave_sum(sa[i]) * (1.f / 512), q0 = wave_sum(sq[i]) * (1.f / 512);
      sd[i] = ln_std(q0 - m0 * m0); mb[i] = rbf(m0 + piv[i]);
    }
#pragma unroll
    for (int i = 0; i < 2; ++i) {
      const float rs = __builtin_amdgcn_rcpf(sd[i]), off = (piv[i] - mb[i]) * rs;
      stat[i] = BC(unsigned, sd[i]) | (BC(unsigned, mb[i]) >> 16);
      v4u uu;
#pragma unroll
      for (int e = 0; e < 4; ++e) { const f2 u = v[i][e] * f2{rs, rs} + f2{off, off}; uu[e] = pk_f16(u[0], u[1]); }
      *(LAS v4u*)(L + L_LN + (7 - (2 * w + i)) * LN_TS + lane * 16) = uu;
    }
  }
  __builtin_amdgcn_raw_buffer_store_b64(stat, rws, lane == 0 ? WS_SD + cw * 4096 + b * 16 + sh * 8 : 0x7ffffff0u, 0, 16);
  BAR();
  {
    LAS unsigned char* p = L + L_LN + q4 * LN_TS + w * 128 + g4 * 32 + p4 * 8;
    const unsigned o = b * 8192 + w * 1024 + lane * 16;
#pragma unroll
    for (int rep = 0; rep < 2; ++rep) {
      const v4u t = cat(tr8(p + rep * 512), tr8(p + rep * 512 + 4 * LN_TS));
      __builtin_amdgcn_raw_buffer_store_b128(t, rws, o + WS_UT + rep * 4096, 0, 16);
    }
  }
  {
    const unsigned t2 = stA + (8 * (lane >> 5) + q4) * 128 + (((2 * (g4 & 1) + (p4 >> 1)) ^ ((q4 >> 1) << 2)) * 16) + 8 * (p4 & 1);
    asm volatile("s_waitcnt vmcnt(8)\n\tds_read_b64_tr_b16 v[240:241], %6\n\tds_read_b64_tr_b16 v[242:243], %6 offset:512\n\t"
                 "ds_read_b64_tr_b16 v[244:245], %7\n\tds_read_b64_tr_b16 v[246:247], %7 offset:512"
                 : "+v"(pAre), "+v"(pAim), "+v"(pBre), "+v"(pBim), "+v"(pCre), "+v"(pCim) : "v"(t2), "v"(t2 ^ 64u) : "memory");
  }
  const float dt = rbf(__builtin_amdgcn_exp2f((cw ? bhi(pdt) : blo(pdt)) * 1.44269504088896341f));
  const f2 dt2 = {dt, dt};
  const f2 ar = (bpair(pAre) * dt2) * f2{1.44269504088896341f, 1.44269504088896341f};
  const f2 ai = (bpair(pAim) * dt2) * f2{0.15915494309189535f, 0.15915494309189535f};
  C2 cd = cmul(C2{bpair(pCre), bpair(pCim)}, C2{bpair(pBre) * dt2, bpair(pBim) * dt2});
  asm volatile("s_waitcnt lgkmcnt(0)\n\tbuffer_store_dwordx4 v[240:243], %0, %1, 0 offen sc1\n\tbuffer_store_dwordx4 v[244:247], %0, %1, 0 offen offset:1024 sc1\n\t"
               "buffer_store_dwordx4 v[248:251], %2, %1, 0 offen sc1"
               :: "v"(b < 224 ? WS_PW + ((pu & 7) * 112 + (pu >> 3)) * 2048 + lane * 16 : 0x7ffffff0u), "s"(rws),
                  "v"(lane < 48 ? WS_PB + pm * 49152 + (b >> 1) * 384 + pl * 16 : 0x7ffffff0u), "v"(cd.re), "v"(cd.im), "v"(ar), "v"(ai) : "memory");
  const float dks = cw ? bhi(pdk) : blo(pdk);
  unsigned e0, e1;
  asm volatile("v_readfirstlane_b32 %0, v234\n\tv_readfirstlane_b32 %1, v235\n\ts_nop 3" : "=s"(e0), "=s"(e1) : "v"(dks) : "memory");
  const u64 E = (u64)e1 << 32 | e0;
  const u64 nonce = mix64(E);
  const unsigned nlo = (unsigned)nonce, nhi = (unsigned)(nonce >> 32);

  const int ex = max(-100, __builtin_amdgcn_frexp_expf(wave_max_nonneg(fmaxf(fmaxf(fabsf(cd.re[0]), fabsf(cd.re[1])), fmaxf(fabsf(cd.im[0]), fabsf(cd.im[1]))))));
  const float isc = __builtin_amdgcn_ldexpf(1.f, ex - 6);
  const unsigned mK = w * 8704;
  if (sh == 0) {
    unsigned tmp;
    const float sc = __builtin_amdgcn_ldexpf(1.f, 6 - ex);
    const C2 e1 = cexpn(ar, ai, 1.f), e256 = cexpn(ar, ai, 256.f);
    asm volatile(
      "s_mov_b32 m0, %[mK]\n\t"
      "v_mov_b64 v[148:149], %[cr]\n\t"
      "v_mov_b64 v[150:151], %[ci]\n\t"
      "v_mov_b64 v[128:129], %[er]\n\t"
      "v_mov_b64 v[130:131], %[ei]\n\t"
      "v_mov_b64 v[144:145], %[fr]\n\t"
      "v_mov_b64 v[146:147], %[fi]\n\t"
      "v_cvt_pk_f16_f32 v172, v148, -v150\n\t"
      "v_cvt_pk_f16_f32 v173, v149, -v151\n\t"
      "v_pk_mul_f32 v[168:169], v[150:151], v[146:147]\n\t"
      "v_pk_mul_f32 v[170:171], v[150:151], v[144:145]\n\t"
      "v_pk_fma_f32 v[162:163], v[148:149], v[146:147], v[170:171]\n\t"
      "v_pk_fma_f32 v[160:161], v[148:149], v[144:145], v[168:169] neg_lo:[0,0,1] neg_hi:[0,0,1]\n\t"
      "v_cvt_pk_f16_f32 v174, v160, -v162\n\t"
      "v_cvt_pk_f16_f32 v175, v161, -v163\n\t"
      "ds_write_addtid_b32 v172 offset:0\n\t"
      "ds_write_addtid_b32 v173 offset:256\n\t"
      "ds_write_addtid_b32 v174 offset:4352\n\t"
      "ds_write_addtid_b32 v175 offset:4608\n\t"
      "v_pk_mul_f32 v[168:169], v[150:151], v[130:131]\n\t"
      "v_pk_mul_f32 v[170:171], v[150:151], v[128:129]\n\t"
      "v_pk_fma_f32 v[150:151], v[148:149], v[130:131], v[170:171]\n\t"
      "v_pk_fma_f32 v[148:149], v[148:149], v[128:129], v[168:169] neg_lo:[0,0,1] neg_hi:[0,0,1]\n\t"
      "v_cvt_pk_f16_f32 v172, v148, -v150\n\t"
      "v_cvt_pk_f16_f32 v173, v149, -v151\n\t"
      "v_pk_mul_f32 v[168:169], v[130:131], v[130:131]\n\t"
      "v_pk_mul_f32 v[170:171], v[130:131], v[128:129]\n\t"
      "v_pk_fma_f32 v[134:135], v[128:129], v[130:131], v[170:171]\n\t"
      "v_pk_fma_f32 v[132:133], v[128:129], v[128:129], v[168:169] neg_lo:[0,0,1] neg_hi:[0,0,1]\n\t"
      "v_pk_mul_f32 v[168:169], v[162:163], v[134:135]\n\t"
      "v_pk_mul_f32 v[170:171], v[162:163], v[132:133]\n\t"
      "v_pk_fma_f32 v[154:155], v[160:161], v[134:135], v[170:171]\n\t"
      "v_pk_fma_f32 v[152:153], v[160:161], v[132:133], v[168:169] neg_lo:[0,0,1] neg_hi:[0,0,1]\n\t"
      "v_cvt_pk_f16_f32 v174, v152, -v154\n\t"
      "v_cvt_pk_f16_f32 v175, v153, -v155\n\t"
      "ds_write_addtid_b32 v172 offset:544\n\t"
      "ds_write_addtid_b32 v173 offset:800\n\t"
      "ds_write_addtid_b32 v174 offset:4896\n\t"
      "ds_write_addtid_b32 v175 offset:5152\n\t"
      "v_pk_mul_f32 v[168:169], v[162:163], v[146:147]\n\t"
      "v_pk_mul_f32 v[170:171], v[162:163], v[144:145]\n\t"
      "v_pk_fma_f32 v[162:163], v[160:161], v[146:147], v[170:171]\n\t"
      "v_pk_fma_f32 v[160:161], v[160:161], v[144:145], v[168:169] neg_lo:[0,0,1] neg_hi:[0,0,1]\n\t"
      "v_cvt_pk_f16_f32 v172, v160, -v162\n\t"
      "v_cvt_pk_f16_f32 v173, v161, -v163\n\t"
      "v_pk_mul_f32 v[168:169], v[150:151], v[130:131]\n\t"
      "v_pk_mul_f32 v[170:171], v[150:151], v[128:129]\n\t"
      "v_pk_fma_f32 v[150:151], v[148:149], v[130:131], v[170:171]\n\t"
      "v_pk_fma_f32 v[148:149], v[148:149], v[128:129], v[168:169] neg_lo:[0,0,1] neg_hi:[0,0,1]\n\t"
      "v_cvt_pk_f16_f32 v174, v148, -v150\n\t"
      "v_cvt_pk_f16_f32 v175, v149, -v151\n\t"
      "ds_write_addtid_b32 v172 offset:35360\n\t"
      "ds_write_addtid_b32 v173 offset:35616\n\t"
      "ds_write_addtid_b32 v174 offset:1088\n\t"
      "ds_write_addtid_b32 v175 offset:1344\n\t"
      "v_pk_mul_f32 v[168:169], v[154:155], v[134:135]\n\t"
      "v_pk_mul_f32 v[170:171], v[154:155], v[132:133]\n\t"
      "v_pk_fma_f32 v[154:155], v[152:153], v[134:135], v[170:171]\n\t"
      "v_pk_fma_f32 v[152:153], v[152:153], v[132:133], v[168:169] neg_lo:[0,0,1] neg_hi:[0,0,1]\n\t"
      "v_cvt_pk_f16_f32 v172, v152, -v154\n\t"
      "v_cvt_pk_f16_f32 v173, v153, -v155\n\t"
      "v_pk_mul_f32 v[168:169], v[134:135], v[134:135]\n\t"
      "v_pk_mul_f32 v[170:171], v[134:135], v[132:133]\n\t"
      "v_pk_fma_f32 v[138:139], v[132:133], v[134:135], v[170:171]\n\t"
      "v_pk_fma_f32 v[136:137], v[132:133], v[132:133], v[168:169] neg_lo:[0,0,1] neg_hi:[0,0,1]\n\t"
      "v_pk_mul_f32 v[168:169], v[162:163], v[138:139]\n\t"
      "v_pk_mul_f32 v[170:171], v[162:163], v[136:137]\n\t"
      "v_pk_fma_f32 v[166:167], v[160:161], v[138:139], v[170:171]\n\t"
      "v_pk_fma_f32 v[164:165], v[160:161], v[136:137], v[168:169] neg_lo:[0,0,1] neg_hi:[0,0,1]\n\t"
      "v_cvt_pk_f16_f32 v174, v164, -v166\n\t"
      "v_cvt_pk_f16_f32 v175, v165, -v167\n\t"
      "ds_write_addtid_b32 v172 offset:5440\n\t"
      "ds_write_addtid_b32 v173 offset:5696\n\t"
      "ds_write_addtid_b32 v174 offset:35904\n\t"
      "ds_write_addtid_b32 v175 offset:36160\n\t"
      "v_pk_mul_f32 v[168:169], v[150:151], v[130:131]\n\t"
      "v_pk_mul_f32 v[170:171], v[150:151], v[128:129]\n\t"
      "v_pk_fma_f32 v[150:151], v[148:149], v[130:131], v[170:171]\n\t"
      "v_pk_fma_f32 v[148:149], v[148:149], v[128:129], v[168:169] neg_lo:[0,0,1] neg_hi:[0,0,1]\n\t"
      "v_cvt_pk_f16_f32 v172, v148, -v150\n\t"
      "v_cvt_pk_f16_f32 v173, v149, -v151\n\t"
      "v_pk_mul_f32 v[168:169], v[138:139], v[138:139]\n\t"
      "v_pk_mul_f32 v[170:171], v[138:139], v[136:137]\n\t"
      "v_pk_fma_f32 v[142:143], v[136:137], v[138:139], v[170:171]\n\t"
      "v_pk_fma_f32 v[140:141], v[136:137], v[136:137], v[168:169] neg_lo:[0,0,1] neg_hi:[0,0,1]\n\t"
      "v_pk_mul_f32 v[168:169], v[162:163], v[142:143]\n\t"
      "v_pk_mul_f32 v[170:171], v[162:163], v[140:141]\n\t"
      "v_pk_fma_f32 v[166:167], v[160:161], v[142:143], v[170:171]\n\t"
      "v_pk_fma_f32 v[164:165], v[160:161], v[140:141], v[168:169] neg_lo:[0,0,1] neg_hi:[0,0,1]\n\t"
      "v_cvt_pk_f16_f32 v174, v164, -v166\n\t"
      "v_cvt_pk_f16_f32 v175, v165, -v167\n\t"
      "ds_write_addtid_b32 v172 offset:1632\n\t"
      "ds_write_addtid_b32 v173 offset:1888\n\t"
      "ds_write_addtid_b32 v174 offset:36448\n\t"
      "ds_write_addtid_b32 v175 offset:36704\n\t"
      "v_pk_mul_f32 v[168:169], v[154:155], v[134:135]\n\t"
      "v_pk_mul_f32 v[170:171], v[154:155], v[132:133]\n\t"
      "v_pk_fma_f32 v[154:155], v[152:153], v[134:135], v[170:171]\n\t"
      "v_pk_fma_f32 v[152:153], v[152:153], v[132:133], v[168:169] neg_lo:[0,0,1] neg_hi:[0,0,1]\n\t"
      "v_cvt_pk_f16_f32 v172, v152, -v154\n\t"
      "v_cvt_pk_f16_f32 v173, v153, -v155\n\t"
      "v_pk_mul_f32 v[168:169], v[162:163], v[146:147]\n\t"
      "v_pk_mul_f32 v[170:171], v[162:163], v[144:145]\n\t"
      "v_pk_fma_f32 v[162:163], v[160:161], v[146:147], v[170:171]\n\t"
      "v_pk_fma_f32 v[160:161], v[160:161], v[144:145], v[168:169] neg_lo:[0,0,1] neg_hi:[0,0,1]\n\t"
      "v_cvt_pk_f16_f32 v174, v160, -v162\n\t"
      "v_cvt_pk_f16_f32 v175, v161, -v163\n\t"
      "ds_write_addtid_b32 v172 offset:5984\n\t"
      "ds_write_addtid_b32 v173 offset:6240\n\t"
      "ds_write_addtid_b32 v174 offset:37536\n\t"
      "ds_write_addtid_b32 v175 offset:37792\n\t"
      "v_pk_mul_f32 v[168:169], v[150:151], v[130:131]\n\t"
      "v_pk_mul_f32 v[170:171], v[150:151], v[128:129]\n\t"
      "v_pk_fma_f32 v[150:151], v[148:149], v[130:131], v[170:171]\n\t"
      "v_pk_fma_f32 v[148:149], v[148:149], v[128:129], v[168:169] neg_lo:[0,0,1] neg_hi:[0,0,1]\n\t"
      "v_cvt_pk_f16_f32 v172, v148, -v150\n\t"
      "v_cvt_pk_f16_f32 v173, v149, -v151\n\t"
      "v_cvt_pk_f16_f32 v174, v148, -v150\n\t"
      "v_cvt_pk_f16_f32 v175, v149, -v151\n\t"
      "ds_write_addtid_b32 v172 offset:2176\n\t"
      "ds_write_addtid_b32 v173 offset:2432\n\t"
      "ds_write_addtid_b32 v174 offset:34816\n\t"
      "ds_write_addtid_b32 v175 offset:35072\n\t"
        : [tmp] "=&s"(tmp)
        : [mK] "s"(mK), [cr] "v"(cd.re * f2{sc, sc}), [ci] "v"(cd.im * f2{sc, sc}), [er] "v"(e1.re), [ei] "v"(e1.im), [fr] "v"(e256.re), [fi] "v"(e256.im),
          [mA] "s"(stA), [sA] "s"(sA), [vA] "v"(vA), [rs] "s"(rw1) : "memory", "scc");
  } else {
    unsigned tmp;
    const C2 e8 = cexpn(ar, ai, 8.f);
    asm volatile(
      "s_mov_b32 m0, %[mK]\n\t"
      "v_mov_b64 v[152:153], %[er]\n\t"
      "v_mov_b64 v[154:155], %[ei]\n\t"
      "v_mov_b32 v148, 1.0\n\t"
      "v_mov_b32 v149, 1.0\n\t"
      "v_mov_b32 v150, 0\n\t"
      "v_mov_b32 v151, 0\n\t"
      "v_pk_mul_f32 v[168:169], v[154:155], v[154:155]\n\t"
      "v_pk_mul_f32 v[170:171], v[154:155], v[152:153]\n\t"
      "v_pk_fma_f32 v[130:131], v[152:153], v[154:155], v[170:171]\n\t"
      "v_pk_fma_f32 v[128:129], v[152:153], v[152:153], v[168:169] neg_lo:[0,0,1] neg_hi:[0,0,1]\n\t"
      "v_cvt_pk_f16_f32 v172, v148, v150\n\t"
      "v_cvt_pk_f16_f32 v173, v149, v151\n\t"
      "v_cvt_pk_f16_f32 v174, v152, v154\n\t"
      "v_cvt_pk_f16_f32 v175, v153, v155\n\t"
      "ds_write_addtid_b32 v172 offset:0\n\t"
      "ds_write_addtid_b32 v173 offset:256\n\t"
      "ds_write_addtid_b32 v174 offset:544\n\t"
      "ds_write_addtid_b32 v175 offset:800\n\t"
      "v_pk_mul_f32 v[168:169], v[150:151], v[130:131]\n\t"
      "v_pk_mul_f32 v[170:171], v[150:151], v[128:129]\n\t"
      "v_pk_fma_f32 v[150:151], v[148:149], v[130:131], v[170:171]\n\t"
      "v_pk_fma_f32 v[148:149], v[148:149], v[128:129], v[168:169] neg_lo:[0,0,1] neg_hi:[0,0,1]\n\t"
      "v_cvt_pk_f16_f32 v172, v148, v150\n\t"
      "v_cvt_pk_f16_f32 v173, v149, v151\n\t"
      "v_pk_mul_f32 v[168:169], v[154:155], v[130:131]\n\t"
      "v_pk_mul_f32 v[170:171], v[154:155], v[128:129]\n\t"
      "v_pk_fma_f32 v[154:155], v[152:153], v[130:131], v[170:171]\n\t"
      "v_pk_fma_f32 v[152:153], v[152:153], v[128:129], v[168:169] neg_lo:[0,0,1] neg_hi:[0,0,1]\n\t"
      "v_cvt_pk_f16_f32 v174, v152, v154\n\t"
      "v_cvt_pk_f16_f32 v175, v153, v155\n\t"
      "ds_write_addtid_b32 v172 offset:1088\n\t"
      "ds_write_addtid_b32 v173 offset:1344\n\t"
      "ds_write_addtid_b32 v174 offset:1632\n\t"
      "ds_write_addtid_b32 v175 offset:1888\n\t"
      "v_pk_mul_f32 v[168:169], v[150:151], v[130:131]\n\t"
      "v_pk_mul_f32 v[170:171], v[150:151], v[128:129]\n\t"
      "v_pk_fma_f32 v[150:151], v[148:149], v[130:131], v[170:171]\n\t"
      "v_pk_fma_f32 v[148:149], v[148:149], v[128:129], v[168:169] neg_lo:[0,0,1] neg_hi:[0,0,1]\n\t"
      "v_cvt_pk_f16_f32 v172, v148, v150\n\t"
      "v_cvt_pk_f16_f32 v173, v149, v151\n\t"
      "v_pk_mul_f32 v[168:169], v[154:155], v[130:131]\n\t"
      "v_pk_mul_f32 v[170:171], v[154:155], v[128:129]\n\t"
      "v_pk_fma_f32 v[154:155], v[152:153], v[130:131], v[170:171]\n\t"
      "v_pk_fma_f32 v[152:153], v[152:153], v[128:129], v[168:169] neg_lo:[0,0,1] neg_hi:[0,0,1]\n\t"
      "v_cvt_pk_f16_f32 v174, v152, v154\n\t"
      "v_cvt_pk_f16_f32 v175, v153, v155\n\t"
      "ds_write_addtid_b32 v172 offset:2176\n\t"
      "ds_write_addtid_b32 v173 offset:2432\n\t"
      "ds_write_addtid_b32 v174 offset:2720\n\t"
      "ds_write_addtid_b32 v175 offset:2976\n\t"
      "v_pk_mul_f32 v[168:169], v[150:151], v[130:131]\n\t"
      "v_pk_mul_f32 v[170:171], v[150:151], v[128:129]\n\t"
      "v_pk_fma_f32 v[150:151], v[148:149], v[130:131], v[170:171]\n\t"
      "v_pk_fma_f32 v[148:149], v[148:149], v[128:129], v[168:169] neg_lo:[0,0,1] neg_hi:[0,0,1]\n\t"
      "v_cvt_pk_f16_f32 v172, v148, v150\n\t"
      "v_cvt_pk_f16_f32 v173, v149, v151\n\t"
      "v_pk_mul_f32 v[168:169], v[154:155], v[130:131]\n\t"
      "v_pk_mul_f32 v[170:171], v[154:155], v[128:129]\n\t"
      "v_pk_fma_f32 v[154:155], v[152:153], v[130:131], v[170:171]\n\t"
      "v_pk_fma_f32 v[152:153], v[152:153], v[128:129], v[168:169] neg_lo:[0,0,1] neg_hi:[0,0,1]\n\t"
      "v_cvt_pk_f16_f32 v174, v152, v154\n\t"
      "v_cvt_pk_f16_f32 v175, v153, v155\n\t"
      "ds_write_addtid_b32 v172 offset:3264\n\t"
      "ds_write_addtid_b32 v173 offset:3520\n\t"
      "ds_write_addtid_b32 v174 offset:3808\n\t"
      "ds_write_addtid_b32 v175 offset:4064\n\t"
      "v_pk_mul_f32 v[168:169], v[150:151], v[130:131]\n\t"
      "v_pk_mul_f32 v[170:171], v[150:151], v[128:129]\n\t"
      "v_pk_fma_f32 v[150:151], v[148:149], v[130:131], v[170:171]\n\t"
      "v_pk_fma_f32 v[148:149], v[148:149], v[128:129], v[168:169] neg_lo:[0,0,1] neg_hi:[0,0,1]\n\t"
      "v_cvt_pk_f16_f32 v172, v148, v150\n\t"
      "v_cvt_pk_f16_f32 v173, v149, v151\n\t"
      "v_pk_mul_f32 v[168:169], v[154:155], v[130:131]\n\t"
      "v_pk_mul_f32 v[170:171], v[154:155], v[128:129]\n\t"
      "v_pk_fma_f32 v[154:155], v[152:153], v[130:131], v[170:171]\n\t"
      "v_pk_fma_f32 v[152:153], v[152:153], v[128:129], v[168:169] neg_lo:[0,0,1] neg_hi:[0,0,1]\n\t"
      "v_cvt_pk_f16_f32 v174, v152, v154\n\t"
      "v_cvt_pk_f16_f32 v175, v153, v155\n\t"
      "ds_write_addtid_b32 v172 offset:4352\n\t"
      "ds_write_addtid_b32 v173 offset:4608\n\t"
      "ds_write_addtid_b32 v174 offset:4896\n\t"
      "ds_write_addtid_b32 v175 offset:5152\n\t"
      "v_pk_mul_f32 v[168:169], v[150:151], v[130:131]\n\t"
      "v_pk_mul_f32 v[170:171], v[150:151], v[128:129]\n\t"
      "v_pk_fma_f32 v[150:151], v[148:149], v[130:131], v[170:171]\n\t"
      "v_pk_fma_f32 v[148:149], v[148:149], v[128:129], v[168:169] neg_lo:[0,0,1] neg_hi:[0,0,1]\n\t"
      "v_cvt_pk_f16_f32 v172, v148, v150\n\t"
      "v_cvt_pk_f16_f32 v173, v149, v151\n\t"
      "v_pk_mul_f32 v[168:169], v[154:155], v[130:131]\n\t"
      "v_pk_mul_f32 v[170:171], v[154:155], v[128:129]\n\t"
      "v_pk_fma_f32 v[154:155], v[152:153], v[130:131], v[170:171]\n\t"
      "v_pk_fma_f32 v[152:153], v[152:153], v[128:129], v[168:169] neg_lo:[0,0,1] neg_hi:[0,0,1]\n\t"
      "v_cvt_pk_f16_f32 v174, v152, v154\n\t"
      "v_cvt_pk_f16_f32 v175, v153, v155\n\t"
      "ds_write_addtid_b32 v172 offset:5440\n\t"
      "ds_write_addtid_b32 v173 offset:5696\n\t"
      "ds_write_addtid_b32 v174 offset:5984\n\t"
      "ds_write_addtid_b32 v175 offset:6240\n\t"
      "v_pk_mul_f32 v[168:169], v[150:151], v[130:131]\n\t"
      "v_pk_mul_f32 v[170:171], v[150:151], v[128:129]\n\t"
      "v_pk_fma_f32 v[150:151], v[148:149], v[130:131], v[170:171]\n\t"
      "v_pk_fma_f32 v[148:149], v[148:149], v[128:129], v[168:169] neg_lo:[0,0,1] neg_hi:[0,0,1]\n\t"
      "v_cvt_pk_f16_f32 v172, v148, v150\n\t"
      "v_cvt_pk_f16_f32 v173, v149, v151\n\t"
      "v_pk_mul_f32 v[168:169], v[154:155], v[130:131]\n\t"
      "v_pk_mul_f32 v[170:171], v[154:155], v[128:129]\n\t"
      "v_pk_fma_f32 v[154:155], v[152:153], v[130:131], v[170:171]\n\t"
      "v_pk_fma_f32 v[152:153], v[152:153], v[128:129], v[168:169] neg_lo:[0,0,1] neg_hi:[0,0,1]\n\t"
      "v_cvt_pk_f16_f32 v174, v152, v154\n\t"
      "v_cvt_pk_f16_f32 v175, v153, v155\n\t"
      "ds_write_addtid_b32 v172 offset:6528\n\t"
      "ds_write_addtid_b32 v173 offset:6784\n\t"
      "ds_write_addtid_b32 v174 offset:7072\n\t"
      "ds_write_addtid_b32 v175 offset:7328\n\t"
      "v_pk_mul_f32 v[168:169], v[150:151], v[130:131]\n\t"
      "v_pk_mul_f32 v[170:171], v[150:151], v[128:129]\n\t"
      "v_pk_fma_f32 v[150:151], v[148:149], v[130:131], v[170:171]\n\t"
      "v_pk_fma_f32 v[148:149], v[148:149], v[128:129], v[168:169] neg_lo:[0,0,1] neg_hi:[0,0,1]\n\t"
      "v_cvt_pk_f16_f32 v172, v148, v150\n\t"
      "v_cvt_pk_f16_f32 v173, v149, v151\n\t"
      "v_pk_mul_f32 v[168:169], v[154:155], v[130:131]\n\t"
      "v_pk_mul_f32 v[170:171], v[154:155], v[128:129]\n\t"
      "v_pk_fma_f32 v[154:155], v[152:153], v[130:131], v[170:171]\n\t"
      "v_pk_fma_f32 v[152:153], v[152:153], v[128:129], v[168:169] neg_lo:[0,0,1] neg_hi:[0,0,1]\n\t"
      "v_cvt_pk_f16_f32 v174, v152, v154\n\t"
      "v_cvt_pk_f16_f32 v175, v153, v155\n\t"
      "ds_write_addtid_b32 v172 offset:7616\n\t"
      "ds_write_addtid_b32 v173 offset:7872\n\t"
      "ds_write_addtid_b32 v174 offset:8160\n\t"
      "ds_write_addtid_b32 v175 offset:8416\n\t"
      "v_pk_mul_f32 v[168:169], v[150:151], v[130:131]\n\t"
      "v_pk_mul_f32 v[170:171], v[150:151], v[128:129]\n\t"
      "v_pk_fma_f32 v[150:151], v[148:149], v[130:131], v[170:171]\n\t"
      "v_pk_fma_f32 v[148:149], v[148:149], v[128:129], v[168:169] neg_lo:[0,0,1] neg_hi:[0,0,1]\n\t"
      "v_cvt_pk_f16_f32 v172, v148, v150\n\t"
      "v_cvt_pk_f16_f32 v173, v149, v151\n\t"
      "v_pk_mul_f32 v[168:169], v[154:155], v[130:131]\n\t"
      "v_pk_mul_f32 v[170:171], v[154:155], v[128:129]\n\t"
      "v_pk_fma_f32 v[154:155], v[152:153], v[130:131], v[170:171]\n\t"
      "v_pk_fma_f32 v[152:153], v[152:153], v[128:129], v[168:169] neg_lo:[0,0,1] neg_hi:[0,0,1]\n\t"
      "v_cvt_pk_f16_f32 v174, v152, v154\n\t"
      "v_cvt_pk_f16_f32 v175, v153, v155\n\t"
      "ds_write_addtid_b32 v172 offset:34816\n\t"
      "ds_write_addtid_b32 v173 offset:35072\n\t"
      "ds_write_addtid_b32 v174 offset:35360\n\t"
      "ds_write_addtid_b32 v175 offset:35616\n\t"
        : [tmp] "=&s"(tmp) : [mK] "s"(mK), [er] "v"(e8.re), [ei] "v"(e8.im), [mA] "s"(stA), [sA] "s"(sA), [vA] "v"(vA), [rs] "s"(rw1) : "memory", "scc");
  }
  VMWAIT(0);
  BAR();
  if (w == 0 && lane < 8) __hip_atomic_store((GAS u64*)(ws + WS_FA + lane * 2048 + b * 8), nonce, RLX);
  {
    unsigned tmp;
    if (sh == 0)
      asm volatile(
      "s_mov_b32 m0, %[mK]\n\t"
      "v_pk_mul_f32 v[168:169], v[162:163], v[138:139]\n\t"
      "v_pk_mul_f32 v[170:171], v[162:163], v[136:137]\n\t"
      "v_pk_fma_f32 v[166:167], v[160:161], v[138:139], v[170:171]\n\t"
      "v_pk_fma_f32 v[164:165], v[160:161], v[136:137], v[168:169] neg_lo:[0,0,1] neg_hi:[0,0,1]\n\t"
      "v_cvt_pk_f16_f32 v172, v164, -v166\n\t"
      "v_cvt_pk_f16_f32 v173, v165, -v167\n\t"
      "v_pk_mul_f32 v[168:169], v[162:163], v[142:143]\n\t"
      "v_pk_mul_f32 v[170:171], v[162:163], v[140:141]\n\t"
      "v_pk_fma_f32 v[166:167], v[160:161], v[142:143], v[170:171]\n\t"
      "v_pk_fma_f32 v[164:165], v[160:161], v[140:141], v[168:169] neg_lo:[0,0,1] neg_hi:[0,0,1]\n\t"
      "v_cvt_pk_f16_f32 v174, v164, -v166\n\t"
      "v_cvt_pk_f16_f32 v175, v165, -v167\n\t"
      "ds_write_addtid_b32 v172 offset:38080\n\t"
      "ds_write_addtid_b32 v173 offset:38336\n\t"
      "ds_write_addtid_b32 v174 offset:38624\n\t"
      "ds_write_addtid_b32 v175 offset:38880\n\t"
      "s_add_u32 m0, %[mA], 0\n\t"
      "s_add_u32 %[tmp], %[sA], 0\n\t"
      "buffer_load_dwordx4 %[vA], %[rs], %[tmp] offen lds\n\t"
      "s_mov_b32 m0, %[mK]\n\t"
      "v_pk_mul_f32 v[168:169], v[154:155], v[134:135]\n\t"
      "v_pk_mul_f32 v[170:171], v[154:155], v[132:133]\n\t"
      "v_pk_fma_f32 v[154:155], v[152:153], v[134:135], v[170:171]\n\t"
      "v_pk_fma_f32 v[152:153], v[152:153], v[132:133], v[168:169] neg_lo:[0,0,1] neg_hi:[0,0,1]\n\t"
      "v_cvt_pk_f16_f32 v172, v152, -v154\n\t"
      "v_cvt_pk_f16_f32 v173, v153, -v155\n\t"
      "v_pk_mul_f32 v[168:169], v[162:163], v[146:147]\n\t"
      "v_pk_mul_f32 v[170:171], v[162:163], v[144:145]\n\t"
      "v_pk_fma_f32 v[162:163], v[160:161], v[146:147], v[170:171]\n\t"
      "v_pk_fma_f32 v[160:161], v[160:161], v[144:145], v[168:169] neg_lo:[0,0,1] neg_hi:[0,0,1]\n\t"
      "v_cvt_pk_f16_f32 v174, v160, -v162\n\t"
      "v_cvt_pk_f16_f32 v175, v161, -v163\n\t"
      "ds_write_addtid_b32 v172 offset:39168\n\t"
      "ds_write_addtid_b32 v173 offset:39424\n\t"
      "ds_write_addtid_b32 v174 offset:40256\n\t"
      "ds_write_addtid_b32 v175 offset:40512\n\t"
      "s_add_u32 m0, %[mA], 1024\n\t"
      "s_add_u32 %[tmp], %[sA], 28672\n\t"
      "buffer_load_dwordx4 %[vA], %[rs], %[tmp] offen lds\n\t"
      "s_mov_b32 m0, %[mK]\n\t"
      "v_pk_mul_f32 v[168:169], v[150:151], v[130:131]\n\t"
      "v_pk_mul_f32 v[170:171], v[150:151], v[128:129]\n\t"
      "v_pk_fma_f32 v[150:151], v[148:149], v[130:131], v[170:171]\n\t"
      "v_pk_fma_f32 v[148:149], v[148:149], v[128:129], v[168:169] neg_lo:[0,0,1] neg_hi:[0,0,1]\n\t"
      "v_cvt_pk_f16_f32 v172, v148, -v150\n\t"
      "v_cvt_pk_f16_f32 v173, v149, -v151\n\t"
      "v_pk_mul_f32 v[168:169], v[162:163], v[142:143]\n\t"
      "v_pk_mul_f32 v[170:171], v[162:163], v[140:141]\n\t"
      "v_pk_fma_f32 v[166:167], v[160:161], v[142:143], v[170:171]\n\t"
      "v_pk_fma_f32 v[164:165], v[160:161], v[140:141], v[168:169] neg_lo:[0,0,1] neg_hi:[0,0,1]\n\t"
      "v_cvt_pk_f16_f32 v174, v164, -v166\n\t"
      "v_cvt_pk_f16_f32 v175, v165, -v167\n\t"
      "ds_write_addtid_b32 v172 offset:2720\n\t"
      "ds_write_addtid_b32 v173 offset:2976\n\t"
      "ds_write_addtid_b32 v174 offset:40800\n\t"
      "ds_write_addtid_b32 v175 offset:41056\n\t"
      "s_add_u32 m0, %[mA], 2048\n\t"
      "s_add_u32 %[tmp], %[sA], 57344\n\t"
      "buffer_load_dwordx4 %[vA], %[rs], %[tmp] offen lds\n\t"
      "s_mov_b32 m0, %[mK]\n\t"
      "v_pk_mul_f32 v[168:169], v[162:163], v[146:147]\n\t"
      "v_pk_mul_f32 v[170:171], v[162:163], v[144:145]\n\t"
      "v_pk_fma_f32 v[162:163], v[160:161], v[146:147], v[170:171]\n\t"
      "v_pk_fma_f32 v[160:161], v[160:161], v[144:145], v[168:169] neg_lo:[0,0,1] neg_hi:[0,0,1]\n\t"
      "v_cvt_pk_f16_f32 v172, v160, -v162\n\t"
      "v_cvt_pk_f16_f32 v173, v161, -v163\n\t"
      "v_pk_mul_f32 v[168:169], v[150:151], v[130:131]\n\t"
      "v_pk_mul_f32 v[170:171], v[150:151], v[128:129]\n\t"
      "v_pk_fma_f32 v[150:151], v[148:149], v[130:131], v[170:171]\n\t"
      "v_pk_fma_f32 v[148:149], v[148:149], v[128:129], v[168:169] neg_lo:[0,0,1] neg_hi:[0,0,1]\n\t"
      "v_cvt_pk_f16_f32 v174, v148, -v150\n\t"
      "v_cvt_pk_f16_f32 v175, v149, -v151\n\t"
      "ds_write_addtid_b32 v172 offset:6528\n\t"
      "ds_write_addtid_b32 v173 offset:6784\n\t"
      "ds_write_addtid_b32 v174 offset:3264\n\t"
      "ds_write_addtid_b32 v175 offset:3520\n\t"
      "s_add_u32 m0, %[mA], 3072\n\t"
      "s_add_u32 %[tmp], %[sA], 86016\n\t"
      "buffer_load_dwordx4 %[vA], %[rs], %[tmp] offen lds\n\t"
      "s_mov_b32 m0, %[mK]\n\t"
      "v_pk_mul_f32 v[168:169], v[162:163], v[142:143]\n\t"
      "v_pk_mul_f32 v[170:171], v[162:163], v[140:141]\n\t"
      "v_pk_fma_f32 v[166:167], v[160:161], v[142:143], v[170:171]\n\t"
      "v_pk_fma_f32 v[164:165], v[160:161], v[140:141], v[168:169] neg_lo:[0,0,1] neg_hi:[0,0,1]\n\t"
      "v_cvt_pk_f16_f32 v172, v164, -v166\n\t"
      "v_cvt_pk_f16_f32 v173, v165, -v167\n\t"
      "v_pk_mul_f32 v[168:169], v[162:163], v[146:147]\n\t"
      "v_pk_mul_f32 v[170:171], v[162:163], v[144:145]\n\t"
      "v_pk_fma_f32 v[162:163], v[160:161], v[146:147], v[170:171]\n\t"
      "v_pk_fma_f32 v[160:161], v[160:161], v[144:145], v[168:169] neg_lo:[0,0,1] neg_hi:[0,0,1]\n\t"
      "v_cvt_pk_f16_f32 v174, v160, -v162\n\t"
      "v_cvt_pk_f16_f32 v175, v161, -v163\n\t"
      "ds_write_addtid_b32 v172 offset:7072\n\t"
      "ds_write_addtid_b32 v173 offset:7328\n\t"
      "ds_write_addtid_b32 v174 offset:42432\n\t"
      "ds_write_addtid_b32 v175 offset:42688\n\t"
      "s_add_u32 m0, %[mA], 4096\n\t"
      "s_add_u32 %[tmp], %[sA], 114688\n\t"
      "buffer_load_dwordx4 %[vA], %[rs], %[tmp] offen lds\n\t"
      "s_mov_b32 m0, %[mK]\n\t"
      "v_pk_mul_f32 v[168:169], v[150:151], v[130:131]\n\t"
      "v_pk_mul_f32 v[170:171], v[150:151], v[128:129]\n\t"
      "v_pk_fma_f32 v[150:151], v[148:149], v[130:131], v[170:171]\n\t"
      "v_pk_fma_f32 v[148:149], v[148:149], v[128:129], v[168:169] neg_lo:[0,0,1] neg_hi:[0,0,1]\n\t"
      "v_cvt_pk_f16_f32 v172, v148, -v150\n\t"
      "v_cvt_pk_f16_f32 v173, v149, -v151\n\t"
      "v_pk_mul_f32 v[168:169], v[162:163], v[142:143]\n\t"
      "v_pk_mul_f32 v[170:171], v[162:163], v[140:141]\n\t"
      "v_pk_fma_f32 v[166:167], v[160:161], v[142:143], v[170:171]\n\t"
      "v_pk_fma_f32 v[164:165], v[160:161], v[140:141], v[168:169] neg_lo:[0,0,1] neg_hi:[0,0,1]\n\t"
      "v_cvt_pk_f16_f32 v174, v164, -v166\n\t"
      "v_cvt_pk_f16_f32 v175, v165, -v167\n\t"
      "ds_write_addtid_b32 v172 offset:3808\n\t"
      "ds_write_addtid_b32 v173 offset:4064\n\t"
      "ds_write_addtid_b32 v174 offset:42976\n\t"
      "ds_write_addtid_b32 v175 offset:43232\n\t"
      "s_add_u32 m0, %[mA], 5120\n\t"
      "s_add_u32 %[tmp], %[sA], 143360\n\t"
      "buffer_load_dwordx4 %[vA], %[rs], %[tmp] offen lds\n\t"
      "s_mov_b32 m0, %[mK]\n\t"
      "v_pk_mul_f32 v[168:169], v[162:163], v[146:147]\n\t"
      "v_pk_mul_f32 v[170:171], v[162:163], v[144:145]\n\t"
      "v_pk_fma_f32 v[162:163], v[160:161], v[146:147], v[170:171]\n\t"
      "v_pk_fma_f32 v[160:161], v[160:161], v[144:145], v[168:169] neg_lo:[0,0,1] neg_hi:[0,0,1]\n\t"
      "v_cvt_pk_f16_f32 v172, v160, -v162\n\t"
      "v_cvt_pk_f16_f32 v173, v161, -v163\n\t"
      "v_pk_mul_f32 v[168:169], v[150:151], v[130:131]\n\t"
      "v_pk_mul_f32 v[170:171], v[150:151], v[128:129]\n\t"
      "v_pk_fma_f32 v[150:151], v[148:149], v[130:131], v[170:171]\n\t"
      "v_pk_fma_f32 v[148:149], v[148:149], v[128:129], v[168:169] neg_lo:[0,0,1] neg_hi:[0,0,1]\n\t"
      "v_cvt_pk_f16_f32 v174, v148, -v150\n\t"
      "v_cvt_pk_f16_f32 v175, v149, -v151\n\t"
      "ds_write_addtid_b32 v172 offset:7616\n\t"
      "ds_write_addtid_b32 v173 offset:7872\n\t"
      "ds_write_addtid_b32 v174 offset:36992\n\t"
      "ds_write_addtid_b32 v175 offset:37248\n\t"
      "s_add_u32 m0, %[mA], 6144\n\t"
      "s_add_u32 %[tmp], %[sA], 172032\n\t"
      "buffer_load_dwordx4 %[vA], %[rs], %[tmp] offen lds\n\t"
      "s_mov_b32 m0, %[mK]\n\t"
      "v_pk_mul_f32 v[168:169], v[162:163], v[142:143]\n\t"
      "v_pk_mul_f32 v[170:171], v[162:163], v[140:141]\n\t"
      "v_pk_fma_f32 v[166:167], v[160:161], v[142:143], v[170:171]\n\t"
      "v_pk_fma_f32 v[164:165], v[160:161], v[140:141], v[168:169] neg_lo:[0,0,1] neg_hi:[0,0,1]\n\t"
      "v_cvt_pk_f16_f32 v172, v164, -v166\n\t"
      "v_cvt_pk_f16_f32 v173, v165, -v167\n\t"
      "ds_write_addtid_b32 v172 offset:8160\n\t"
      "ds_write_addtid_b32 v173 offset:8416\n\t"
      "s_add_u32 m0, %[mA], 7168\n\t"
      "s_add_u32 %[tmp], %[sA], 200704\n\t"
      "buffer_load_dwordx4 %[vA], %[rs], %[tmp] offen lds\n\t"
      "s_mov_b32 m0, %[mK]\n\t"
          : [tmp] "=&s"(tmp) : [mK] "s"(mK), [mA] "s"(stA), [sA] "s"(sA), [vA] "v"(vA), [rs] "s"(rw1),  [rws] "s"(rws) : "memory", "scc");
    else
      asm volatile(
      "s_mov_b32 m0, %[mK]\n\t"
      "v_pk_mul_f32 v[168:169], v[150:151], v[130:131]\n\t"
      "v_pk_mul_f32 v[170:171], v[150:151], v[128:129]\n\t"
      "v_pk_fma_f32 v[150:151], v[148:149], v[130:131], v[170:171]\n\t"
      "v_pk_fma_f32 v[148:149], v[148:149], v[128:129], v[168:169] neg_lo:[0,0,1] neg_hi:[0,0,1]\n\t"
      "v_cvt_pk_f16_f32 v172, v148, v150\n\t"
      "v_cvt_pk_f16_f32 v173, v149, v151\n\t"
      "v_pk_mul_f32 v[168:169], v[154:155], v[130:131]\n\t"
      "v_pk_mul_f32 v[170:171], v[154:155], v[128:129]\n\t"
      "v_pk_fma_f32 v[154:155], v[152:153], v[130:131], v[170:171]\n\t"
      "v_pk_fma_f32 v[152:153], v[152:153], v[128:129], v[168:169] neg_lo:[0,0,1] neg_hi:[0,0,1]\n\t"
      "v_cvt_pk_f16_f32 v174, v152, v154\n\t"
      "v_cvt_pk_f16_f32 v175, v153, v155\n\t"
      "ds_write_addtid_b32 v172 offset:35904\n\t"
      "ds_write_addtid_b32 v173 offset:36160\n\t"
      "ds_write_addtid_b32 v174 offset:36448\n\t"
      "ds_write_addtid_b32 v175 offset:36704\n\t"
      "s_add_u32 m0, %[mA], 0\n\t"
      "s_add_u32 %[tmp], %[sA], 0\n\t"
      "buffer_load_dwordx4 %[vA], %[rs], %[tmp] offen lds\n\t"
      "s_add_u32 m0, %[mA], 1024\n\t"
      "s_add_u32 %[tmp], %[sA], 28672\n\t"
      "buffer_load_dwordx4 %[vA], %[rs], %[tmp] offen lds\n\t"
      "s_mov_b32 m0, %[mK]\n\t"
      "v_pk_mul_f32 v[168:169], v[150:151], v[130:131]\n\t"
      "v_pk_mul_f32 v[170:171], v[150:151], v[128:129]\n\t"
      "v_pk_fma_f32 v[150:151], v[148:149], v[130:131], v[170:171]\n\t"
      "v_pk_fma_f32 v[148:149], v[148:149], v[128:129], v[168:169] neg_lo:[0,0,1] neg_hi:[0,0,1]\n\t"
      "v_cvt_pk_f16_f32 v172, v148, v150\n\t"
      "v_cvt_pk_f16_f32 v173, v149, v151\n\t"
      "v_pk_mul_f32 v[168:169], v[154:155], v[130:131]\n\t"
      "v_pk_mul_f32 v[170:171], v[154:155], v[128:129]\n\t"
      "v_pk_fma_f32 v[154:155], v[152:153], v[130:131], v[170:171]\n\t"
      "v_pk_fma_f32 v[152:153], v[152:153], v[128:129], v[168:169] neg_lo:[0,0,1] neg_hi:[0,0,1]\n\t"
      "v_cvt_pk_f16_f32 v174, v152, v154\n\t"
      "v_cvt_pk_f16_f32 v175, v153, v155\n\t"
      "ds_write_addtid_b32 v172 offset:36992\n\t"
      "ds_write_addtid_b32 v173 offset:37248\n\t"
      "ds_write_addtid_b32 v174 offset:37536\n\t"
      "ds_write_addtid_b32 v175 offset:37792\n\t"
      "s_add_u32 m0, %[mA], 2048\n\t"
      "s_add_u32 %[tmp], %[sA], 57344\n\t"
      "buffer_load_dwordx4 %[vA], %[rs], %[tmp] offen lds\n\t"
      "s_mov_b32 m0, %[mK]\n\t"
      "v_pk_mul_f32 v[168:169], v[150:151], v[130:131]\n\t"
      "v_pk_mul_f32 v[170:171], v[150:151], v[128:129]\n\t"
      "v_pk_fma_f32 v[150:151], v[148:149], v[130:131], v[170:171]\n\t"
      "v_pk_fma_f32 v[148:149], v[148:149], v[128:129], v[168:169] neg_lo:[0,0,1] neg_hi:[0,0,1]\n\t"
      "v_cvt_pk_f16_f32 v172, v148, v150\n\t"
      "v_cvt_pk_f16_f32 v173, v149, v151\n\t"
      "v_pk_mul_f32 v[168:169], v[154:155], v[130:131]\n\t"
      "v_pk_mul_f32 v[170:171], v[154:155], v[128:129]\n\t"
      "v_pk_fma_f32 v[154:155], v[152:153], v[130:131], v[170:171]\n\t"
      "v_pk_fma_f32 v[152:153], v[152:153], v[128:129], v[168:169] neg_lo:[0,0,1] neg_hi:[0,0,1]\n\t"
      "v_cvt_pk_f16_f32 v174, v152, v154\n\t"
      "v_cvt_pk_f16_f32 v175, v153, v155\n\t"
      "ds_write_addtid_b32 v172 offset:38080\n\t"
      "ds_write_addtid_b32 v173 offset:38336\n\t"
      "ds_write_addtid_b32 v174 offset:38624\n\t"
      "ds_write_addtid_b32 v175 offset:38880\n\t"
      "s_add_u32 m0, %[mA], 3072\n\t"
      "s_add_u32 %[tmp], %[sA], 86016\n\t"
      "buffer_load_dwordx4 %[vA], %[rs], %[tmp] offen lds\n\t"
      "s_mov_b32 m0, %[mK]\n\t"
      "v_pk_mul_f32 v[168:169], v[150:151], v[130:131]\n\t"
      "v_pk_mul_f32 v[170:171], v[150:151], v[128:129]\n\t"
      "v_pk_fma_f32 v[150:151], v[148:149], v[130:131], v[170:171]\n\t"
      "v_pk_fma_f32 v[148:149], v[148:149], v[128:129], v[168:169] neg_lo:[0,0,1] neg_hi:[0,0,1]\n\t"
      "v_cvt_pk_f16_f32 v172, v148, v150\n\t"
      "v_cvt_pk_f16_f32 v173, v149, v151\n\t"
      "v_pk_mul_f32 v[168:169], v[154:155], v[130:131]\n\t"
      "v_pk_mul_f32 v[170:171], v[154:155], v[128:129]\n\t"
      "v_pk_fma_f32 v[154:155], v[152:153], v[130:131], v[170:171]\n\t"
      "v_pk_fma_f32 v[152:153], v[152:153], v[128:129], v[168:169] neg_lo:[0,0,1] neg_hi:[0,0,1]\n\t"
      "v_cvt_pk_f16_f32 v174, v152, v154\n\t"
      "v_cvt_pk_f16_f32 v175, v153, v155\n\t"
      "ds_write_addtid_b32 v172 offset:39168\n\t"
      "ds_write_addtid_b32 v173 offset:39424\n\t"
      "ds_write_addtid_b32 v174 offset:39712\n\t"
      "ds_write_addtid_b32 v175 offset:39968\n\t"
      "s_add_u32 m0, %[mA], 4096\n\t"
      "s_add_u32 %[tmp], %[sA], 114688\n\t"
      "buffer_load_dwordx4 %[vA], %[rs], %[tmp] offen lds\n\t"
      "s_mov_b32 m0, %[mK]\n\t"
      "v_pk_mul_f32 v[168:169], v[150:151], v[130:131]\n\t"
      "v_pk_mul_f32 v[170:171], v[150:151], v[128:129]\n\t"
      "v_pk_fma_f32 v[150:151], v[148:149], v[130:131], v[170:171]\n\t"
      "v_pk_fma_f32 v[148:149], v[148:149], v[128:129], v[168:169] neg_lo:[0,0,1] neg_hi:[0,0,1]\n\t"
      "v_cvt_pk_f16_f32 v172, v148, v150\n\t"
      "v_cvt_pk_f16_f32 v173, v149, v151\n\t"
      "v_pk_mul_f32 v[168:169], v[154:155], v[130:131]\n\t"
      "v_pk_mul_f32 v[170:171], v[154:155], v[128:129]\n\t"
      "v_pk_fma_f32 v[154:155], v[152:153], v[130:131], v[170:171]\n\t"
      "v_pk_fma_f32 v[152:153], v[152:153], v[128:129], v[168:169] neg_lo:[0,0,1] neg_hi:[0,0,1]\n\t"
      "v_cvt_pk_f16_f32 v174, v152, v154\n\t"
      "v_cvt_pk_f16_f32 v175, v153, v155\n\t"
      "ds_write_addtid_b32 v172 offset:40256\n\t"
      "ds_write_addtid_b32 v173 offset:40512\n\t"
      "ds_write_addtid_b32 v174 offset:40800\n\t"
      "ds_write_addtid_b32 v175 offset:41056\n\t"
      "s_add_u32 m0, %[mA], 5120\n\t"
      "s_add_u32 %[tmp], %[sA], 143360\n\t"
      "buffer_load_dwordx4 %[vA], %[rs], %[tmp] offen lds\n\t"
      "s_mov_b32 m0, %[mK]\n\t"
      "v_pk_mul_f32 v[168:169], v[150:151], v[130:131]\n\t"
      "v_pk_mul_f32 v[170:171], v[150:151], v[128:129]\n\t"
      "v_pk_fma_f32 v[150:151], v[148:149], v[130:131], v[170:171]\n\t"
      "v_pk_fma_f32 v[148:149], v[148:149], v[128:129], v[168:169] neg_lo:[0,0,1] neg_hi:[0,0,1]\n\t"
      "v_cvt_pk_f16_f32 v172, v148, v150\n\t"
      "v_cvt_pk_f16_f32 v173, v149, v151\n\t"
      "v_pk_mul_f32 v[168:169], v[154:155], v[130:131]\n\t"
      "v_pk_mul_f32 v[170:171], v[154:155], v[128:129]\n\t"
      "v_pk_fma_f32 v[154:155], v[152:153], v[130:131], v[170:171]\n\t"
      "v_pk_fma_f32 v[152:153], v[152:153], v[128:129], v[168:169] neg_lo:[0,0,1] neg_hi:[0,0,1]\n\t"
      "v_cvt_pk_f16_f32 v174, v152, v154\n\t"
      "v_cvt_pk_f16_f32 v175, v153, v155\n\t"
      "ds_write_addtid_b32 v172 offset:41344\n\t"
      "ds_write_addtid_b32 v173 offset:41600\n\t"
      "ds_write_addtid_b32 v174 offset:41888\n\t"
      "ds_write_addtid_b32 v175 offset:42144\n\t"
      "s_add_u32 m0, %[mA], 6144\n\t"
      "s_add_u32 %[tmp], %[sA], 172032\n\t"
      "buffer_load_dwordx4 %[vA], %[rs], %[tmp] offen lds\n\t"
      "s_mov_b32 m0, %[mK]\n\t"
      "v_pk_mul_f32 v[168:169], v[150:151], v[130:131]\n\t"
      "v_pk_mul_f32 v[170:171], v[150:151], v[128:129]\n\t"
      "v_pk_fma_f32 v[150:151], v[148:149], v[130:131], v[170:171]\n\t"
      "v_pk_fma_f32 v[148:149], v[148:149], v[128:129], v[168:169] neg_lo:[0,0,1] neg_hi:[0,0,1]\n\t"
      "v_cvt_pk_f16_f32 v172, v148, v150\n\t"
      "v_cvt_pk_f16_f32 v173, v149, v151\n\t"
      "v_pk_mul_f32 v[168:169], v[154:155], v[130:131]\n\t"
      "v_pk_mul_f32 v[170:171], v[154:155], v[128:129]\n\t"
      "v_pk_fma_f32 v[154:155], v[152:153], v[130:131], v[170:171]\n\t"
      "v_pk_fma_f32 v[152:153], v[152:153], v[128:129], v[168:169] neg_lo:[0,0,1] neg_hi:[0,0,1]\n\t"
      "v_cvt_pk_f16_f32 v174, v152, v154\n\t"
      "v_cvt_pk_f16_f32 v175, v153, v155\n\t"
      "ds_write_addtid_b32 v172 offset:42432\n\t"
      "ds_write_addtid_b32 v173 offset:42688\n\t"
      "ds_write_addtid_b32 v174 offset:42976\n\t"
      "ds_write_addtid_b32 v175 offset:43232\n\t"
      "s_add_u32 m0, %[mA], 7168\n\t"
      "s_add_u32 %[tmp], %[sA], 200704\n\t"
      "buffer_load_dwordx4 %[vA], %[rs], %[tmp] offen lds\n\t"
      "s_mov_b32 m0, %[mK]\n\t"
          : [tmp] "=&s"(tmp) : [mK] "s"(mK), [mA] "s"(stA), [sA] "s"(sA), [vA] "v"(vA), [rs] "s"(rw1),  [rws] "s"(rws) : "memory", "scc");
  }
  asm volatile(
      "s_waitcnt lgkmcnt(0)\n\t"
      "s_barrier\n\t"
      "ds_read_b128 v[128:131], %[pa] offset:0\n\t"
      "ds_read_b128 v[136:139], %[pb] offset:0\n\t"
      "ds_read_b128 v[132:135], %[pa] offset:34816\n\t"
      "ds_read_b128 v[140:143], %[pa] offset:64\n\t"
      "ds_read_b128 v[148:151], %[pb] offset:64\n\t"
      "ds_read_b128 v[144:147], %[pa] offset:34880\n\t"
      "ds_read_b128 v[152:155], %[pa] offset:128\n\t"
      "ds_read_b128 v[160:163], %[pb] offset:128\n\t"
      "ds_read_b128 v[156:159], %[pa] offset:34944\n\t"
      "ds_read_b128 v[164:167], %[pa] offset:192\n\t"
      "ds_read_b128 v[172:175], %[pb] offset:192\n\t"
      "ds_read_b128 v[168:171], %[pa] offset:35008\n\t"
      "ds_read_b128 v[176:179], %[pa] offset:256\n\t"
      "ds_read_b128 v[184:187], %[pb] offset:256\n\t"
      "ds_read_b128 v[180:183], %[pa] offset:35072\n\t"
      "s_waitcnt lgkmcnt(12)\n\t"
      "v_mfma_f32_16x16x32_f16 v[224:227], v[128:131], v[136:139], 0\n\t"
      "v_mfma_f32_16x16x32_f16 v[228:231], v[132:135], v[136:139], 0\n\t"
      "ds_read_b128 v[188:191], %[pa] offset:320\n\t"
      "ds_read_b128 v[196:199], %[pb] offset:320\n\t"
      "ds_read_b128 v[192:195], %[pa] offset:35136\n\t"
      "s_waitcnt lgkmcnt(12)\n\t"
      "v_mfma_f32_16x16x32_f16 v[224:227], v[140:143], v[148:151], v[224:227]\n\t"
      "v_mfma_f32_16x16x32_f16 v[228:231], v[144:147], v[148:151], v[228:231]\n\t"
      "ds_read_b128 v[200:203], %[pa] offset:384\n\t"
      "ds_read_b128 v[208:211], %[pb] offset:384\n\t"
      "ds_read_b128 v[204:207], %[pa] offset:35200\n\t"
      "s_waitcnt lgkmcnt(12)\n\t"
      "v_mfma_f32_16x16x32_f16 v[224:227], v[152:155], v[160:163], v[224:227]\n\t"
      "v_mfma_f32_16x16x32_f16 v[228:231], v[156:159], v[160:163], v[228:231]\n\t"
      "ds_read_b128 v[212:215], %[pa] offset:448\n\t"
      "ds_read_b128 v[220:223], %[pb] offset:448\n\t"
      "ds_read_b128 v[216:219], %[pa] offset:35264\n\t"
      "s_waitcnt lgkmcnt(12)\n\t"
      "v_mfma_f32_16x16x32_f16 v[224:227], v[164:167], v[172:175], v[224:227]\n\t"
      "v_mfma_f32_16x16x32_f16 v[228:231], v[168:171], v[172:175], v[228:231]\n\t"
      "s_waitcnt lgkmcnt(9)\n\t"
      "v_mfma_f32_16x16x32_f16 v[224:227], v[176:179], v[184:187], v[224:227]\n\t"
      "v_mfma_f32_16x16x32_f16 v[228:231], v[180:183], v[184:187], v[228:231]\n\t"
      "s_waitcnt lgkmcnt(6)\n\t"
      "v_mfma_f32_16x16x32_f16 v[224:227], v[188:191], v[196:199], v[224:227]\n\t"
      "v_mfma_f32_16x16x32_f16 v[228:231], v[192:195], v[196:199], v[228:231]\n\t"
      "s_waitcnt lgkmcnt(3)\n\t"
      "v_mfma_f32_16x16x32_f16 v[224:227], v[200:203], v[208:211], v[224:227]\n\t"
      "v_mfma_f32_16x16x32_f16 v[228:231], v[204:207], v[208:211], v[228:231]\n\t"
      "s_waitcnt lgkmcnt(0)\n\t"
      "v_mfma_f32_16x16x32_f16 v[224:227], v[212:215], v[220:223], v[224:227]\n\t"
      "v_mfma_f32_16x16x32_f16 v[228:231], v[216:219], v[220:223], v[228:231]\n\t"
      "s_nop 3\n\t"
      "s_barrier\n\t"
      :: [pa] "v"(2 * cw * 8704 + r16 * 544 + g4 * 16), [pb] "v"((2 * cw + 1) * 8704 + sh * 34816 + r16 * 544 + g4 * 16) : "memory");
  const unsigned vfa = WS_FA + (b & 7) * 2048 + lane * 16;
  if (w == 3) poll_sample(rws, vfa);
  {
    const unsigned kp = L_KC + cw * KC_CH + 32 + (16 * sh + r16) * 16;
    asm volatile(
      "s_mov_b32 vcc_lo, 0xaaaaaaaa\n\t"
      "s_mov_b32 vcc_hi, 0xaaaaaaaa\n\t"
      "v_mov_b32 v156, 0\n\t"
      "v_mov_b32 v157, 0\n\t"
      "v_mov_b32 v158, 0\n\t"
      "v_mov_b32 v159, 0\n\t"
      "s_mov_b32 exec_lo, -1\n\t"
      "s_mov_b32 exec_hi, 0\n\t"
      "v_cvt_pk_f16_f32 v136, v224, v225\n\t"
      "v_cvt_pk_f16_f32 v137, v226, v227\n\t"
      "v_cvt_pk_f16_f32 v140, v225, v226\n\t"
      "v_cvt_pk_f16_f32 v141, v227, v228\n\t"
      "v_cvt_pk_f16_f32 v128, v229, v229\n\t"
      "v_cvt_pk_f16_f32 v129, v229, v230\n\t"
      "v_cvt_pk_f16_f32 v130, v230, v230\n\t"
      "v_cvt_pk_f16_f32 v131, v231, v231\n\t"
      "v_cvt_pk_f16_f32 v132, v229, v229\n\t"
      "v_cvt_pk_f16_f32 v133, v230, v230\n\t"
      "v_cvt_pk_f16_f32 v134, v230, v231\n\t"
      "v_cvt_pk_f16_f32 v135, v231, v231\n\t"
      "ds_write_b64 %[kb], v[136:137]\n\t"
      "ds_write_b64 %[kb], v[140:141] offset:4160\n\t"
      "s_mov_b32 exec_lo, %[z0]\n\t"
      "v_cvt_pk_f16_f32 v159, v156, v224\n\t"
      "s_mov_b32 exec_lo, 0\n\t"
      "s_mov_b32 exec_hi, -1\n\t"
      "v_cndmask_b32 v152, v230, v231, vcc\n\t"
      "v_cvt_pk_f16_f32 v136, v230, v230\n\t"
      "v_cvt_pk_f16_f32 v137, v230, v230\n\t"
      "v_cvt_pk_f16_f32 v139, v231, v231\n\t"
      "v_cvt_pk_f16_f32 v140, v230, v230\n\t"
      "v_cvt_pk_f16_f32 v142, v231, v231\n\t"
      "v_cvt_pk_f16_f32 v143, v231, v231\n\t"
      "v_cvt_pk_f16_f32 v138, v152, v231\n\t"
      "v_cvt_pk_f16_f32 v141, v230, v152\n\t"
      "s_mov_b32 exec_lo, 0\n\t"
      "s_mov_b32 exec_hi, 0xffff\n\t"
      "v_cvt_pk_f16_f32 v128, v224, v224\n\t"
      "v_cvt_pk_f16_f32 v129, v225, v226\n\t"
      "v_cvt_pk_f16_f32 v130, v226, v226\n\t"
      "v_cvt_pk_f16_f32 v131, v227, v228\n\t"
      "v_cvt_pk_f16_f32 v132, v224, v225\n\t"
      "v_cvt_pk_f16_f32 v133, v226, v226\n\t"
      "v_cvt_pk_f16_f32 v134, v226, v227\n\t"
      "v_cvt_pk_f16_f32 v135, v228, v228\n\t"
      "s_mov_b32 exec_lo, 0\n\t"
      "s_mov_b32 exec_hi, 0xffff0000\n\t"
      "v_cndmask_b32 v152, v224, v225, vcc\n\t"
      "v_cvt_pk_f16_f32 v128, v224, v224\n\t"
      "v_cvt_pk_f16_f32 v129, v224, v224\n\t"
      "v_cvt_pk_f16_f32 v131, v225, v225\n\t"
      "v_cvt_pk_f16_f32 v132, v224, v224\n\t"
      "v_cvt_pk_f16_f32 v134, v225, v225\n\t"
      "v_cvt_pk_f16_f32 v135, v225, v225\n\t"
      "v_cvt_pk_f16_f32 v130, v152, v225\n\t"
      "v_cvt_pk_f16_f32 v133, v224, v152\n\t"
      "v_cndmask_b32 v152, v226, v227, vcc\n\t"
      "v_cvt_pk_f16_f32 v144, v226, v226\n\t"
      "v_cvt_pk_f16_f32 v145, v226, v226\n\t"
      "v_cvt_pk_f16_f32 v147, v227, v227\n\t"
      "v_cvt_pk_f16_f32 v148, v226, v226\n\t"
      "v_cvt_pk_f16_f32 v150, v227, v227\n\t"
      "v_cvt_pk_f16_f32 v151, v227, v227\n\t"
      "v_cvt_pk_f16_f32 v146, v152, v227\n\t"
      "v_cvt_pk_f16_f32 v149, v226, v152\n\t"
      "ds_write_b128 %[ka], v[144:147] offset:1024\n\t"
      "ds_write_b128 %[ka], v[148:151] offset:5184\n\t"
      "s_mov_b32 exec_hi, -1\n\t"
      "ds_write_b128 %[kb], v[136:139]\n\t"
      "ds_write_b128 %[kb], v[140:143] offset:4160\n\t"
      "s_mov_b64 exec, -1\n\t"
      "ds_write_b128 %[ka], v[128:131]\n\t"
      "ds_write_b128 %[ka], v[132:135] offset:4160\n\t"
      "ds_write_b128 %[kz], v[156:159]\n\t"
        :: [ka] "v"(kp + ((0x5132 >> (4 * g4)) & 15) * 512), [kb] "v"(kp + (g4 < 2 ? 8 * g4 : 1024 * g4)), [z0] "s"(sh ^ 1),
           [kz] "v"(lane < 2 ? L_KC + cw * KC_CH + (sh ^ 1) * KC_CP + 16 - 16 * lane : L_UI + cw * UI_CH + sh * UI_H + (128 + r16) * 16)
        : "memory", "vcc");
  }
  if (w == 3) poll_finish(rws, vfa, nlo, nhi);
  BAR();
  if (b == 0 && tid == 0) __hip_atomic_store((GAS u64*)(ws + WS_E), E + 1, RLX);
  const unsigned utb = 64 * w + (lane >> 1);
  asm volatile("buffer_load_dwordx4 v[208:211], %0, %1, 0 offen sc1\n\tbuffer_load_dwordx4 v[212:215], %0, %1, %2 offen sc1"
               :: "v"(WS_UT + utb * 8192 + (ch0 + (lane & 1)) * 16), "s"(rws), "s"(32 * 8192) : "memory");
  const unsigned kq = L_KC + cw * KC_CH + (~r16 & 1) * KC_CP + 256 * sh + 16 * g4 + ((r16 + 1) >> 1) * 4;
  {
    unsigned tmp, tw;
    asm volatile(
      "ds_read2_b32 v[128:129], %[k0] offset0:0 offset1:1\n\t"
      "s_add_u32 m0, %[mA], 8192\n\t"
      "s_add_u32 %[tmp], %[sA], 229376\n\t"
      "buffer_load_dwordx4 %[vA], %[rs], %[tmp] offen lds\n\t"
      "ds_read2_b32 v[130:131], %[k0] offset0:2 offset1:3\n\t"
      "ds_read2_b32 v[132:133], %[k0] offset0:128 offset1:129\n\t"
      "ds_read2_b32 v[134:135], %[k0] offset0:130 offset1:131\n\t"
      "ds_read2_b32 v[136:137], %[k1] offset0:0 offset1:1\n\t"
      "ds_read2_b32 v[138:139], %[k1] offset0:2 offset1:3\n\t"
      "ds_read2_b32 v[140:141], %[k1] offset0:128 offset1:129\n\t"
      "ds_read2_b32 v[142:143], %[k1] offset0:130 offset1:131\n\t"
      "ds_read2_b32 v[144:145], %[k2] offset0:0 offset1:1\n\t"
      "s_add_u32 m0, %[mA], 9216\n\t"
      "s_add_u32 %[tmp], %[sA], 258048\n\t"
      "buffer_load_dwordx4 %[vA], %[rs], %[tmp] offen lds\n\t"
      "ds_read2_b32 v[146:147], %[k2] offset0:2 offset1:3\n\t"
      "ds_read2_b32 v[148:149], %[k2] offset0:128 offset1:129\n\t"
      "ds_read2_b32 v[150:151], %[k2] offset0:130 offset1:131\n\t"
      "ds_read2_b32 v[152:153], %[k3] offset0:0 offset1:1\n\t"
      "ds_read2_b32 v[154:155], %[k3] offset0:2 offset1:3\n\t"
      "ds_read2_b32 v[156:157], %[k3] offset0:128 offset1:129\n\t"
      "ds_read2_b32 v[158:159], %[k3] offset0:130 offset1:131\n\t"
        : [tmp] "=&s"(tmp), [tw] "=&s"(tw)
        : [k0] "v"(kq), [k1] "v"(kq + 1024), [k2] "v"(kq + 2048), [k3] "v"(kq + 3072), [mA] "s"(stA), [sA] "s"(sA), [vA] "v"(vA), [rs] "s"(rw1), [rws] "s"(rws), [sW] "s"(sW), [w0] "v"(vW), [w1] "v"(vW ^ 1024u)
        : "memory", "scc");
  }
  asm volatile("s_waitcnt vmcnt(2)\n\tds_write_b128 %0, v[208:211] offset:256\n\tds_write_b128 %0, v[212:215]"
               :: "v"(L_UI + (lane & 1) * UI_CH + (~utb & 1) * UI_H + ((223 - utb) >> 1) * 16) : "memory");
  BAR();

  {
    unsigned tmp, tw;
    const unsigned tb0 = 32 * (4 * sh + (g4 & 1)) + 2 * r16 + (g4 >> 1), d = ch0 + cw;
    asm volatile(
      "s_cmp_eq_u32 %[sh], 0\n\t"
      "s_cbranch_scc0 CV1%=\n\t"
      "ds_read_b128 v[160:163], %[ub] offset:1792\n\t"
      "ds_read_b128 v[164:167], %[ub] offset:1536\n\t"
      "ds_read_b128 v[168:171], %[ub] offset:1280\n\t"
      "ds_read_b128 v[172:175], %[ub] offset:1024\n\t"
      "ds_read_b128 v[176:179], %[ub] offset:768\n\t"
      "ds_read_b128 v[180:183], %[ub] offset:512\n\t"
      "ds_read_b128 v[184:187], %[ub] offset:256\n\t"
      "ds_read_b128 v[188:191], %[ub] offset:0\n\t"
      "buffer_load_dwordx4 v[96:99], %[vs], %[rws], 0 offen sc1\n\t"
      "buffer_load_dwordx4 v[104:107], %[vs], %[rws], 0 offen offset:1024 sc1\n\t"
      "s_waitcnt lgkmcnt(7)\n\t"
      "v_mfma_f32_16x16x32_f16 v[80:83], v[128:131], v[160:163], 0\n\t"
      "ds_read_b128 v[224:227], %[ub] offset:1824\n\t"
      "s_waitcnt lgkmcnt(7)\n\t"
      "v_mfma_f32_16x16x32_f16 v[84:87], v[128:131], v[164:167], 0\n\t"
      "s_movk_i32 %[tmp], 0x1000\n\t"
      "ds_read2_b32 v[192:193], %[k0] offset0:16 offset1:17\n\t"
      "s_waitcnt lgkmcnt(7)\n\t"
      "v_mfma_f32_16x16x32_f16 v[88:91], v[128:131], v[168:171], 0\n\t"
      "buffer_load_dwordx4 v[100:103], %[vs], %[rws], %[tmp] offen sc1\n\t"
      "ds_read2_b32 v[194:195], %[k0] offset0:18 offset1:19\n\t"
      "s_waitcnt lgkmcnt(7)\n\t"
      "v_mfma_f32_16x16x32_f16 v[92:95], v[128:131], v[172:175], 0\n\t"
      "ds_read_b128 v[228:231], %[ub] offset:1568\n\t"
      "s_waitcnt lgkmcnt(7)\n\t"
      "v_mfma_f32_16x16x32_f16 a[240:243], v[128:131], v[176:179], 0\n\t"
      "buffer_load_dwordx4 v[108:111], %[vs], %[rws], %[tmp] offen offset:1024 sc1\n\t"
      "ds_read2_b32 v[196:197], %[k0] offset0:144 offset1:145\n\t"
      "s_waitcnt lgkmcnt(7)\n\t"
      "v_mfma_f32_16x16x32_f16 a[244:247], v[128:131], v[180:183], 0\n\t"
      "ds_read2_b32 v[198:199], %[k0] offset0:146 offset1:147\n\t"
      "s_waitcnt lgkmcnt(7)\n\t"
      "v_mfma_f32_16x16x32_f16 a[248:251], v[128:131], v[184:187], 0\n\t"
      "ds_read_b128 v[232:235], %[ub] offset:1312\n\t"
      "s_add_u32 m0, %[mA], 10240\n\t"
      "s_waitcnt lgkmcnt(7)\n\t"
      "v_mfma_f32_16x16x32_f16 a[252:255], v[128:131], v[188:191], 0\n\t"
      "ds_read2_b32 v[200:201], %[k1] offset0:16 offset1:17\n\t"
      "s_add_u32 %[tmp], %[sA], 286720\n\t"
      "v_mfma_f32_16x16x32_f16 v[84:87], v[132:135], v[160:163], v[84:87]\n\t"
      "ds_read2_b32 v[202:203], %[k1] offset0:18 offset1:19\n\t"
      "buffer_load_dwordx4 %[vA], %[rs], %[tmp] offen lds\n\t"
      "v_mfma_f32_16x16x32_f16 v[88:91], v[132:135], v[164:167], v[88:91]\n\t"
      "ds_read_b128 v[236:239], %[ub] offset:1056\n\t"
      "v_mfma_f32_16x16x32_f16 v[92:95], v[132:135], v[168:171], v[92:95]\n\t"
      "ds_read2_b32 v[204:205], %[k1] offset0:144 offset1:145\n\t"
      "s_add_u32 %[tw], %[sW], 0\n\t"
      "v_mfma_f32_16x16x32_f16 a[240:243], v[132:135], v[172:175], a[240:243]\n\t"
      "ds_read2_b32 v[206:207], %[k1] offset0:146 offset1:147\n\t"
      "buffer_load_dwordx4 a[16:19], %[w0], %[rws], %[tw] offen offset:0 sc1\n\t"
      "v_mfma_f32_16x16x32_f16 a[244:247], v[132:135], v[176:179], a[244:247]\n\t"
      "ds_read_b128 v[240:243], %[ub] offset:800\n\t"
      "v_mfma_f32_16x16x32_f16 a[248:251], v[132:135], v[180:183], a[248:251]\n\t"
      "ds_read2_b32 v[208:209], %[k2] offset0:16 offset1:17\n\t"
      "v_mfma_f32_16x16x32_f16 a[252:255], v[132:135], v[184:187], a[252:255]\n\t"
      "ds_read2_b32 v[210:211], %[k2] offset0:18 offset1:19\n\t"
      "v_mfma_f32_16x16x32_f16 v[88:91], v[136:139], v[160:163], v[88:91]\n\t"
      "ds_read_b128 v[244:247], %[ub] offset:544\n\t"
      "buffer_load_dwordx4 a[20:23], %[w1], %[rws], %[tw] offen offset:0 sc1\n\t"
      "v_mfma_f32_16x16x32_f16 v[92:95], v[136:139], v[164:167], v[92:95]\n\t"
      "ds_read2_b32 v[212:213], %[k2] offset0:144 offset1:145\n\t"
      "v_mfma_f32_16x16x32_f16 a[240:243], v[136:139], v[168:171], a[240:243]\n\t"
      "ds_read2_b32 v[214:215], %[k2] offset0:146 offset1:147\n\t"
      "v_mfma_f32_16x16x32_f16 a[244:247], v[136:139], v[172:175], a[244:247]\n\t"
      "ds_read_b128 v[248:251], %[ub] offset:288\n\t"
      "v_mfma_f32_16x16x32_f16 a[248:251], v[136:139], v[176:179], a[248:251]\n\t"
      "ds_read2_b32 v[216:217], %[k3] offset0:16 offset1:17\n\t"
      "buffer_load_dwordx4 a[24:27], %[w0], %[rws], %[tw] offen offset:2048 sc1\n\t"
      "v_mfma_f32_16x16x32_f16 a[252:255], v[136:139], v[180:183], a[252:255]\n\t"
      "ds_read2_b32 v[218:219], %[k3] offset0:18 offset1:19\n\t"
      "s_add_u32 m0, %[mA], 11264\n\t"
      "v_mfma_f32_16x16x32_f16 v[92:95], v[140:143], v[160:163], v[92:95]\n\t"
      "ds_read_b128 v[252:255], %[ub] offset:32\n\t"
      "s_add_u32 %[tmp], %[sA], 315392\n\t"
      "v_mfma_f32_16x16x32_f16 a[240:243], v[140:143], v[164:167], a[240:243]\n\t"
      "ds_read2_b32 v[220:221], %[k3] offset0:144 offset1:145\n\t"
      "buffer_load_dwordx4 %[vA], %[rs], %[tmp] offen lds\n\t"
      "v_mfma_f32_16x16x32_f16 a[244:247], v[140:143], v[168:171], a[244:247]\n\t"
      "ds_read2_b32 v[222:223], %[k3] offset0:146 offset1:147\n\t"
      "v_mfma_f32_16x16x32_f16 a[248:251], v[140:143], v[172:175], a[248:251]\n\t"
      "ds_read_b128 v[112:115], %[ua] offset:512\n\t"
      "v_mfma_f32_16x16x32_f16 a[252:255], v[140:143], v[176:179], a[252:255]\n\t"
      "ds_read_b128 v[116:119], %[ua]\n\t"
      "v_mfma_f32_16x16x32_f16 a[240:243], v[144:147], v[160:163], a[240:243]\n\t"
      "buffer_load_dwordx4 a[28:31], %[w1], %[rws], %[tw] offen offset:2048 sc1\n\t"
      "v_mfma_f32_16x16x32_f16 a[244:247], v[144:147], v[164:167], a[244:247]\n\t"
      "v_mfma_f32_16x16x32_f16 a[248:251], v[144:147], v[168:171], a[248:251]\n\t"
      "v_mfma_f32_16x16x32_f16 a[252:255], v[144:147], v[172:175], a[252:255]\n\t"
      "s_add_u32 %[tw], %[sW], 4096\n\t"
      "v_mfma_f32_16x16x32_f16 a[244:247], v[148:151], v[160:163], a[244:247]\n\t"
      "buffer_load_dwordx4 a[32:35], %[w0], %[rws], %[tw] offen offset:0 sc1\n\t"
      "v_mfma_f32_16x16x32_f16 a[248:251], v[148:151], v[164:167], a[248:251]\n\t"
      "s_add_u32 m0, %[mB], 0\n\t"
      "v_mfma_f32_16x16x32_f16 a[252:255], v[148:151], v[168:171], a[252:255]\n\t"
      "s_add_u32 %[tmp], %[sB], 0\n\t"
      "v_mfma_f32_16x16x32_f16 a[248:251], v[152:155], v[160:163], a[248:251]\n\t"
      "buffer_load_dwordx4 %[vl], %[rws], %[tmp] offen sc1 lds\n\t"
      "v_mfma_f32_16x16x32_f16 a[252:255], v[152:155], v[164:167], a[252:255]\n\t"
      "v_mfma_f32_16x16x32_f16 a[252:255], v[156:159], v[160:163], a[252:255]\n\t"
      "s_waitcnt lgkmcnt(0)\n\t"
      "v_mfma_f32_16x16x32_f16 v[80:83], v[192:195], v[224:227], v[80:83]\n\t"
      "ds_read_b128 v[160:163], %[ub] offset:1856\n\t"
      "v_mfma_f32_16x16x32_f16 v[84:87], v[192:195], v[228:231], v[84:87]\n\t"
      "ds_read2_b32 v[128:129], %[k0] offset0:32 offset1:33\n\t"
      "buffer_load_dwordx4 a[36:39], %[w1], %[rws], %[tw] offen offset:0 sc1\n\t"
      "v_mfma_f32_16x16x32_f16 v[88:91], v[192:195], v[232:235], v[88:91]\n\t"
      "ds_read2_b32 v[130:131], %[k0] offset0:34 offset1:35\n\t"
      "v_mfma_f32_16x16x32_f16 v[92:95], v[192:195], v[236:239], v[92:95]\n\t"
      "ds_read_b128 v[164:167], %[ub] offset:1600\n\t"
      "v_mfma_f32_16x16x32_f16 a[240:243], v[192:195], v[240:243], a[240:243]\n\t"
      "ds_read2_b32 v[132:133], %[k0] offset0:160 offset1:161\n\t"
      "v_mfma_f32_16x16x32_f16 a[244:247], v[192:195], v[244:247], a[244:247]\n\t"
      "ds_read2_b32 v[134:135], %[k0] offset0:162 offset1:163\n\t"
      "buffer_load_dwordx4 a[40:43], %[w0], %[rws], %[tw] offen offset:2048 sc1\n\t"
      "v_mfma_f32_16x16x32_f16 a[248:251], v[192:195], v[248:251], a[248:251]\n\t"
      "ds_read_b128 v[168:171], %[ub] offset:1344\n\t"
      "v_mfma_f32_16x16x32_f16 a[252:255], v[192:195], v[252:255], a[252:255]\n\t"
      "ds_read2_b32 v[136:137], %[k1] offset0:32 offset1:33\n\t"
      "v_mfma_f32_16x16x32_f16 v[84:87], v[196:199], v[224:227], v[84:87]\n\t"
      "ds_read2_b32 v[138:139], %[k1] offset0:34 offset1:35\n\t"
      "buffer_load_dwordx4 a[44:47], %[w1], %[rws], %[tw] offen offset:2048 sc1\n\t"
      "v_mfma_f32_16x16x32_f16 v[88:91], v[196:199], v[228:231], v[88:91]\n\t"
      "ds_read_b128 v[172:175], %[ub] offset:1088\n\t"
      "v_mfma_f32_16x16x32_f16 v[92:95], v[196:199], v[232:235], v[92:95]\n\t"
      "ds_read2_b32 v[140:141], %[k1] offset0:160 offset1:161\n\t"
      "s_add_u32 m0, %[mB], 1024\n\t"
      "v_mfma_f32_16x16x32_f16 a[240:243], v[196:199], v[236:239], a[240:243]\n\t"
      "ds_read2_b32 v[142:143], %[k1] offset0:162 offset1:163\n\t"
      "s_add_u32 %[tmp], %[sB], 1024\n\t"
      "v_mfma_f32_16x16x32_f16 a[244:247], v[196:199], v[240:243], a[244:247]\n\t"
      "ds_read_b128 v[176:179], %[ub] offset:832\n\t"
      "buffer_load_dwordx4 %[vl], %[rws], %[tmp] offen sc1 lds\n\t"
      "v_mfma_f32_16x16x32_f16 a[248:251], v[196:199], v[244:247], a[248:251]\n\t"
      "ds_read2_b32 v[144:145], %[k2] offset0:32 offset1:33\n\t"
      "v_mfma_f32_16x16x32_f16 a[252:255], v[196:199], v[248:251], a[252:255]\n\t"
      "ds_read2_b32 v[146:147], %[k2] offset0:34 offset1:35\n\t"
      "v_mfma_f32_16x16x32_f16 v[88:91], v[200:203], v[224:227], v[88:91]\n\t"
      "ds_read_b128 v[180:183], %[ub] offset:576\n\t"
      "s_add_u32 %[tw], %[sW], 8192\n\t"
      "v_mfma_f32_16x16x32_f16 v[92:95], v[200:203], v[228:231], v[92:95]\n\t"
      "ds_read2_b32 v[148:149], %[k2] offset0:160 offset1:161\n\t"
      "buffer_load_dwordx4 a[48:51], %[w0], %[rws], %[tw] offen offset:0 sc1\n\t"
      "v_mfma_f32_16x16x32_f16 a[240:243], v[200:203], v[232:235], a[240:243]\n\t"
      "ds_read2_b32 v[150:151], %[k2] offset0:162 offset1:163\n\t"
      "v_mfma_f32_16x16x32_f16 a[244:247], v[200:203], v[236:239], a[244:247]\n\t"
      "ds_read_b128 v[184:187], %[ub] offset:320\n\t"
      "v_mfma_f32_16x16x32_f16 a[248:251], v[200:203], v[240:243], a[248:251]\n\t"
      "ds_read2_b32 v[152:153], %[k3] offset0:32 offset1:33\n\t"
      "buffer_load_dwordx4 a[52:55], %[w1], %[rws], %[tw] offen offset:0 sc1\n\t"
      "v_mfma_f32_16x16x32_f16 a[252:255], v[200:203], v[244:247], a[252:255]\n\t"
      "ds_read2_b32 v[154:155], %[k3] offset0:34 offset1:35\n\t"
      "v_mfma_f32_16x16x32_f16 v[92:95], v[204:207], v[224:227], v[92:95]\n\t"
      "ds_read_b128 v[188:191], %[ub] offset:64\n\t"
      "s_add_u32 m0, %[mB], 2048\n\t"
      "v_mfma_f32_16x16x32_f16 a[240:243], v[204:207], v[228:231], a[240:243]\n\t"
      "ds_read2_b32 v[156:157], %[k3] offset0:160 offset1:161\n\t"
      "s_add_u32 %[tmp], %[sB], 2048\n\t"
      "v_mfma_f32_16x16x32_f16 a[244:247], v[204:207], v[232:235], a[244:247]\n\t"
      "ds_read2_b32 v[158:159], %[k3] offset0:162 offset1:163\n\t"
      "buffer_load_dwordx4 %[vl], %[rws], %[tmp] offen sc1 lds\n\t"
      "v_mfma_f32_16x16x32_f16 a[248:251], v[204:207], v[236:239], a[248:251]\n\t"
      "v_mfma_f32_16x16x32_f16 a[252:255], v[204:207], v[240:243], a[252:255]\n\t"
      "s_waitcnt vmcnt(15)\n\t"
      "v_mfma_f32_16x16x32_f16 a[240:243], v[208:211], v[224:227], a[240:243]\n\t"
      "v_and_b32 v120, 0xffff0000, v96\n\t"
      "v_lshlrev_b32 v96, 16, v96\n\t"
      "v_mfma_f32_16x16x32_f16 a[244:247], v[208:211], v[228:231], a[244:247]\n\t"
      "buffer_load_dwordx4 a[56:59], %[w0], %[rws], %[tw] offen offset:2048 sc1\n\t"
      "v_add_f32 v120, %[dk], v120\n\t"
      "v_fma_mix_f32 v96, v115, v120, v96 op_sel:[1,0,0] op_sel_hi:[1,0,0]\n\t"
      "v_mfma_f32_16x16x32_f16 a[248:251], v[208:211], v[232:235], a[248:251]\n\t"
      "v_and_b32 v120, 0xffff0000, v97\n\t"
      "v_lshlrev_b32 v97, 16, v97\n\t"
      "v_mfma_f32_16x16x32_f16 a[252:255], v[208:211], v[236:239], a[252:255]\n\t"
      "v_add_f32 v120, %[dk], v120\n\t"
      "v_fma_mix_f32 v97, v115, v120, v97 op_sel:[0,0,0] op_sel_hi:[1,0,0]\n\t"
      "v_mfma_f32_16x16x32_f16 a[244:247], v[212:215], v[224:227], a[244:247]\n\t"
      "buffer_load_dwordx4 a[60:63], %[w1], %[rws], %[tw] offen offset:2048 sc1\n\t"
      "v_and_b32 v120, 0xffff0000, v98\n\t"
      "v_lshlrev_b32 v98, 16, v98\n\t"
      "v_mfma_f32_16x16x32_f16 a[248:251], v[212:215], v[228:231], a[248:251]\n\t"
      "v_add_f32 v120, %[dk], v120\n\t"
      "v_fma_mix_f32 v98, v114, v120, v98 op_sel:[1,0,0] op_sel_hi:[1,0,0]\n\t"
      "v_mfma_f32_16x16x32_f16 a[252:255], v[212:215], v[232:235], a[252:255]\n\t"
      "s_add_u32 m0, %[mB], 3072\n\t"
      "v_and_b32 v120, 0xffff0000, v99\n\t"
      "v_lshlrev_b32 v99, 16, v99\n\t"
      "v_mfma_f32_16x16x32_f16 a[248:251], v[216:219], v[224:227], a[248:251]\n\t"
      "s_add_u32 %[tmp], %[sB], 3072\n\t"
      "v_add_f32 v120, %[dk], v120\n\t"
      "v_fma_mix_f32 v99, v114, v120, v99 op_sel:[0,0,0] op_sel_hi:[1,0,0]\n\t"
      "v_mfma_f32_16x16x32_f16 a[252:255], v[216:219], v[228:231], a[252:255]\n\t"
      "buffer_load_dwordx4 %[vl], %[rws], %[tmp] offen sc1 lds\n\t"
      "v_and_b32 v120, 0xffff0000, v100\n\t"
      "v_lshlrev_b32 v100, 16, v100\n\t"
      "v_mfma_f32_16x16x32_f16 a[252:255], v[220:223], v[224:227], a[252:255]\n\t"
      "v_add_f32 v120, %[dk], v120\n\t"
      "v_fma_mix_f32 v100, v113, v120, v100 op_sel:[1,0,0] op_sel_hi:[1,0,0]\n\t"
      "s_waitcnt lgkmcnt(0)\n\t"
      "v_mfma_f32_16x16x32_f16 v[80:83], v[128:131], v[160:163], v[80:83]\n\t"
      "ds_read_b128 v[224:227], %[ub] offset:1888\n\t"
      "v_mfma_f32_16x16x32_f16 v[84:87], v[128:131], v[164:167], v[84:87]\n\t"
      "ds_read2_b32 v[192:193], %[k0] offset0:48 offset1:49\n\t"
      "s_add_u32 %[tw], %[sW], 12288\n\t"
      "v_mfma_f32_16x16x32_f16 v[88:91], v[128:131], v[168:171], v[88:91]\n\t"
      "ds_read2_b32 v[194:195], %[k0] offset0:50 offset1:51\n\t"
      "buffer_load_dwordx4 a[64:67], %[w0], %[rws], %[tw] offen offset:0 sc1\n\t"
      "v_mfma_f32_16x16x32_f16 v[92:95], v[128:131], v[172:175], v[92:95]\n\t"
      "ds_read_b128 v[228:231], %[ub] offset:1632\n\t"
      "v_mfma_f32_16x16x32_f16 a[240:243], v[128:131], v[176:179], a[240:243]\n\t"
      "ds_read2_b32 v[196:197], %[k0] offset0:176 offset1:177\n\t"
      "v_mfma_f32_16x16x32_f16 a[244:247], v[128:131], v[180:183], a[244:247]\n\t"
      "ds_read2_b32 v[198:199], %[k0] offset0:178 offset1:179\n\t"
      "buffer_load_dwordx4 a[68:71], %[w1], %[rws], %[tw] offen offset:0 sc1\n\t"
      "v_mfma_f32_16x16x32_f16 a[248:251], v[128:131], v[184:187], a[248:251]\n\t"
      "ds_read_b128 v[232:235], %[ub] offset:1376\n\t"
      "v_mfma_f32_16x16x32_f16 a[252:255], v[128:131], v[188:191], a[252:255]\n\t"
      "ds_read2_b32 v[200:201], %[k1] offset0:48 offset1:49\n\t"
      "v_mfma_f32_16x16x32_f16 v[84:87], v[132:135], v[160:163], v[84:87]\n\t"
      "ds_read2_b32 v[202:203], %[k1] offset0:50 offset1:51\n\t"
      "v_mfma_f32_16x16x32_f16 v[88:91], v[132:135], v[164:167], v[88:91]\n\t"
      "ds_read_b128 v[236:239], %[ub] offset:1120\n\t"
      "buffer_load_dwordx4 a[72:75], %[w0], %[rws], %[tw] offen offset:2048 sc1\n\t"
      "v_mfma_f32_16x16x32_f16 v[92:95], v[132:135], v[168:171], v[92:95]\n\t"
      "ds_read2_b32 v[204:205], %[k1] offset0:176 offset1:177\n\t"
      "v_mfma_f32_16x16x32_f16 a[240:243], v[132:135], v[172:175], a[240:243]\n\t"
      "ds_read2_b32 v[206:207], %[k1] offset0:178 offset1:179\n\t"
      "s_add_u32 m0, %[mB], 4096\n\t"
      "v_mfma_f32_16x16x32_f16 a[244:247], v[132:135], v[176:179], a[244:247]\n\t"
      "ds_read_b128 v[240:243], %[ub] offset:864\n\t"
      "s_add_u32 %[tmp], %[sB], 4096\n\t"
      "v_mfma_f32_16x16x32_f16 a[248:251], v[132:135], v[180:183], a[248:251]\n\t"
      "ds_read2_b32 v[208:209], %[k2] offset0:48 offset1:49\n\t"
      "buffer_load_dwordx4 %[vl], %[rws], %[tmp] offen sc1 lds\n\t"
      "v_mfma_f32_16x16x32_f16 a[252:255], v[132:135], v[184:187], a[252:255]\n\t"
      "ds_read2_b32 v[210:211], %[k2] offset0:50 offset1:51\n\t"
      "v_mfma_f32_16x16x32_f16 v[88:91], v[136:139], v[160:163], v[88:91]\n\t"
      "ds_read_b128 v[244:247], %[ub] offset:608\n\t"
      "v_mfma_f32_16x16x32_f16 v[92:95], v[136:139], v[164:167], v[92:95]\n\t"
      "ds_read2_b32 v[212:213], %[k2] offset0:176 offset1:177\n\t"
      "buffer_load_dwordx4 a[76:79], %[w1], %[rws], %[tw] offen offset:2048 sc1\n\t"
      "v_mfma_f32_16x16x32_f16 a[240:243], v[136:139], v[168:171], a[240:243]\n\t"
      "ds_read2_b32 v[214:215], %[k2] offset0:178 offset1:179\n\t"
      "v_mfma_f32_16x16x32_f16 a[244:247], v[136:139], v[172:175], a[244:247]\n\t"
      "ds_read_b128 v[248:251], %[ub] offset:352\n\t"
      "v_mfma_f32_16x16x32_f16 a[248:251], v[136:139], v[176:179], a[248:251]\n\t"
      "ds_read2_b32 v[216:217], %[k3] offset0:48 offset1:49\n\t"
      "s_add_u32 %[tw], %[sW], 16384\n\t"
      "v_mfma_f32_16x16x32_f16 a[252:255], v[136:139], v[180:183], a[252:255]\n\t"
      "ds_read2_b32 v[218:219], %[k3] offset0:50 offset1:51\n\t"
      "buffer_load_dwordx4 a[80:83], %[w0], %[rws], %[tw] offen offset:0 sc1\n\t"
      "v_mfma_f32_16x16x32_f16 v[92:95], v[140:143], v[160:163], v[92:95]\n\t"
      "ds_read_b128 v[252:255], %[ub] offset:96\n\t"
      "v_mfma_f32_16x16x32_f16 a[240:243], v[140:143], v[164:167], a[240:243]\n\t"
      "ds_read2_b32 v[220:221], %[k3] offset0:176 offset1:177\n\t"
      "s_add_u32 m0, %[mB], 5120\n\t"
      "v_mfma_f32_16x16x32_f16 a[244:247], v[140:143], v[168:171], a[244:247]\n\t"
      "ds_read2_b32 v[222:223], %[k3] offset0:178 offset1:179\n\t"
      "s_add_u32 %[tmp], %[sB], 5120\n\t"
      "v_mfma_f32_16x16x32_f16 a[248:251], v[140:143], v[172:175], a[248:251]\n\t"
      "buffer_load_dwordx4 %[vl], %[rws], %[tmp] offen sc1 lds\n\t"
      "v_mfma_f32_16x16x32_f16 a[252:255], v[140:143], v[176:179], a[252:255]\n\t"
      "v_mfma_f32_16x16x32_f16 a[240:243], v[144:147], v[160:163], a[240:243]\n\t"
      "v_and_b32 v120, 0xffff0000, v101\n\t"
      "v_lshlrev_b32 v101, 16, v101\n\t"
      "v_mfma_f32_16x16x32_f16 a[244:247], v[144:147], v[164:167], a[244:247]\n\t"
      "buffer_load_dwordx4 a[84:87], %[w1], %[rws], %[tw] offen offset:0 sc1\n\t"
      "v_add_f32 v120, %[dk], v120\n\t"
      "v_fma_mix_f32 v101, v113, v120, v101 op_sel:[0,0,0] op_sel_hi:[1,0,0]\n\t"
      "v_mfma_f32_16x16x32_f16 a[248:251], v[144:147], v[168:171], a[248:251]\n\t"
      "v_and_b32 v120, 0xffff0000, v102\n\t"
      "v_lshlrev_b32 v102, 16, v102\n\t"
      "v_mfma_f32_16x16x32_f16 a[252:255], v[144:147], v[172:175], a[252:255]\n\t"
      "v_add_f32 v120, %[dk], v120\n\t"
      "v_fma_mix_f32 v102, v112, v120, v102 op_sel:[1,0,0] op_sel_hi:[1,0,0]\n\t"
      "v_mfma_f32_16x16x32_f16 a[244:247], v[148:151], v[160:163], a[244:247]\n\t"
      "v_and_b32 v120, 0xffff0000, v103\n\t"
      "v_lshlrev_b32 v103, 16, v103\n\t"
      "v_mfma_f32_16x16x32_f16 a[248:251], v[148:151], v[164:167], a[248:251]\n\t"
      "buffer_load_dwordx4 a[88:91], %[w0], %[rws], %[tw] offen offset:2048 sc1\n\t"
      "v_add_f32 v120, %[dk], v120\n\t"
      "v_fma_mix_f32 v103, v112, v120, v103 op_sel:[0,0,0] op_sel_hi:[1,0,0]\n\t"
      "v_mfma_f32_16x16x32_f16 a[252:255], v[148:151], v[168:171], a[252:255]\n\t"
      "v_and_b32 v120, 0xffff0000, v104\n\t"
      "v_lshlrev_b32 v104, 16, v104\n\t"
      "v_mfma_f32_16x16x32_f16 a[248:251], v[152:155], v[160:163], a[248:251]\n\t"
      "v_add_f32 v120, %[dk], v120\n\t"
      "v_fma_mix_f32 v104, v119, v120, v104 op_sel:[1,0,0] op_sel_hi:[1,0,0]\n\t"
      "v_mfma_f32_16x16x32_f16 a[252:255], v[152:155], v[164:167], a[252:255]\n\t"
      "v_and_b32 v120, 0xffff0000, v105\n\t"
      "v_lshlrev_b32 v105, 16, v105\n\t"
      "v_mfma_f32_16x16x32_f16 a[252:255], v[156:159], v[160:163], a[252:255]\n\t"
      "buffer_load_dwordx4 a[92:95], %[w1], %[rws], %[tw] offen offset:2048 sc1\n\t"
      "v_add_f32 v120, %[dk], v120\n\t"
      "v_fma_mix_f32 v105, v119, v120, v105 op_sel:[0,0,0] op_sel_hi:[1,0,0]\n\t"
      "s_waitcnt lgkmcnt(0)\n\t"
      "v_mfma_f32_16x16x32_f16 a[240:243], v[192:195], v[240:243], a[240:243]\n\t"
      "s_add_u32 m0, %[mB], 6144\n\t"
      "v_and_b32 v120, 0xffff0000, v106\n\t"
      "v_lshlrev_b32 v106, 16, v106\n\t"
      "v_mfma_f32_16x16x32_f16 a[240:243], v[196:199], v[236:239], a[240:243]\n\t"
      "s_add_u32 %[tmp], %[sB], 6144\n\t"
      "v_add_f32 v120, %[dk], v120\n\t"
      "v_fma_mix_f32 v106, v118, v120, v106 op_sel:[1,0,0] op_sel_hi:[1,0,0]\n\t"
      "v_mfma_f32_16x16x32_f16 a[240:243], v[200:203], v[232:235], a[240:243]\n\t"
      "buffer_load_dwordx4 %[vl], %[rws], %[tmp] offen sc1 lds\n\t"
      "v_and_b32 v120, 0xffff0000, v107\n\t"
      "v_lshlrev_b32 v107, 16, v107\n\t"
      "v_mfma_f32_16x16x32_f16 a[240:243], v[204:207], v[228:231], a[240:243]\n\t"
      "v_add_f32 v120, %[dk], v120\n\t"
      "v_fma_mix_f32 v107, v118, v120, v107 op_sel:[0,0,0] op_sel_hi:[1,0,0]\n\t"
      "v_mfma_f32_16x16x32_f16 a[240:243], v[208:211], v[224:227], a[240:243]\n\t"
      "v_and_b32 v120, 0xffff0000, v108\n\t"
      "v_lshlrev_b32 v108, 16, v108\n\t"
      "v_mfma_f32_16x16x32_f16 a[244:247], v[192:195], v[244:247], a[244:247]\n\t"
      "s_add_u32 %[tw], %[sW], 20480\n\t"
      "v_add_f32 v120, %[dk], v120\n\t"
      "v_fma_mix_f32 v108, v117, v120, v108 op_sel:[1,0,0] op_sel_hi:[1,0,0]\n\t"
      "v_mfma_f32_16x16x32_f16 a[244:247], v[196:199], v[240:243], a[244:247]\n\t"
      "buffer_load_dwordx4 a[96:99], %[w0], %[rws], %[tw] offen offset:0 sc1\n\t"
      "v_and_b32 v120, 0xffff0000, v109\n\t"
      "v_lshlrev_b32 v109, 16, v109\n\t"
      "v_mfma_f32_16x16x32_f16 a[244:247], v[200:203], v[236:239], a[244:247]\n\t"
      "v_add_f32 v120, %[dk], v120\n\t"
      "v_fma_mix_f32 v109, v117, v120, v109 op_sel:[0,0,0] op_sel_hi:[1,0,0]\n\t"
      "v_mfma_f32_16x16x32_f16 a[244:247], v[204:207], v[232:235], a[244:247]\n\t"
      "v_and_b32 v120, 0xffff0000, v110\n\t"
      "v_lshlrev_b32 v110, 16, v110\n\t"
      "v_mfma_f32_16x16x32_f16 a[244:247], v[208:211], v[228:231], a[244:247]\n\t"
      "v_add_f32 v120, %[dk], v120\n\t"
      "v_fma_mix_f32 v110, v116, v120, v110 op_sel:[1,0,0] op_sel_hi:[1,0,0]\n\t"
      "v_mfma_f32_16x16x32_f16 a[244:247], v[212:215], v[224:227], a[244:247]\n\t"
      "buffer_load_dwordx4 a[100:103], %[w1], %[rws], %[tw] offen offset:0 sc1\n\t"
      "v_and_b32 v120, 0xffff0000, v111\n\t"
      "v_lshlrev_b32 v111, 16, v111\n\t"
      "v_mfma_f32_16x16x32_f16 a[248:251], v[192:195], v[248:251], a[248:251]\n\t"
      "s_add_u32 m0, %[mB], 7168\n\t"
      "v_add_f32 v120, %[dk], v120\n\t"
      "v_fma_mix_f32 v111, v116, v120, v111 op_sel:[0,0,0] op_sel_hi:[1,0,0]\n\t"
      "v_mfma_f32_16x16x32_f16 a[248:251], v[196:199], v[244:247], a[248:251]\n\t"
      "s_add_u32 %[tmp], %[sB], 7168\n\t"
      "v_mfma_f32_16x16x32_f16 a[248:251], v[200:203], v[240:243], a[248:251]\n\t"
      "buffer_load_dwordx4 %[vl], %[rws], %[tmp] offen sc1 lds\n\t"
      "ds_write_b128 %[pr], a[240:243] offset:0\n\t"
      "v_mfma_f32_16x16x32_f16 a[248:251], v[204:207], v[236:239], a[248:251]\n\t"
      "v_mfma_f32_16x16x32_f16 a[248:251], v[208:211], v[232:235], a[248:251]\n\t"
      "v_mfma_f32_16x16x32_f16 a[248:251], v[212:215], v[228:231], a[248:251]\n\t"
      "ds_write_b128 %[pr], a[244:247] offset:1024\n\t"
      "v_mfma_f32_16x16x32_f16 a[248:251], v[216:219], v[224:227], a[248:251]\n\t"
      "buffer_load_dwordx4 a[104:107], %[w0], %[rws], %[tw] offen offset:2048 sc1\n\t"
      "v_mfma_f32_16x16x32_f16 a[252:255], v[192:195], v[252:255], a[252:255]\n\t"
      "v_mfma_f32_16x16x32_f16 a[252:255], v[196:199], v[248:251], a[252:255]\n\t"
      "v_mfma_f32_16x16x32_f16 a[252:255], v[200:203], v[244:247], a[252:255]\n\t"
      "v_mfma_f32_16x16x32_f16 a[252:255], v[204:207], v[240:243], a[252:255]\n\t"
      "buffer_load_dwordx4 a[108:111], %[w1], %[rws], %[tw] offen offset:2048 sc1\n\t"
      "v_mfma_f32_16x16x32_f16 a[252:255], v[208:211], v[236:239], a[252:255]\n\t"
      "ds_write_b128 %[pr], a[248:251] offset:2048\n\t"
      "v_mfma_f32_16x16x32_f16 a[252:255], v[212:215], v[232:235], a[252:255]\n\t"
      "v_mfma_f32_16x16x32_f16 a[252:255], v[216:219], v[228:231], a[252:255]\n\t"
      "v_mfma_f32_16x16x32_f16 a[252:255], v[220:223], v[224:227], a[252:255]\n\t"
      "v_mfma_f32_16x16x32_f16 v[80:83], v[192:195], v[224:227], v[80:83]\n\t"
      "v_mfma_f32_16x16x32_f16 v[84:87], v[192:195], v[228:231], v[84:87]\n\t"
      "v_mfma_f32_16x16x32_f16 v[84:87], v[196:199], v[224:227], v[84:87]\n\t"
      "v_mfma_f32_16x16x32_f16 v[88:91], v[192:195], v[232:235], v[88:91]\n\t"
      "v_mfma_f32_16x16x32_f16 v[88:91], v[196:199], v[228:231], v[88:91]\n\t"
      "ds_write_b128 %[pr], a[252:255] offset:3072\n\t"
      "v_mfma_f32_16x16x32_f16 v[88:91], v[200:203], v[224:227], v[88:91]\n\t"
      "v_mfma_f32_16x16x32_f16 v[92:95], v[192:195], v[236:239], v[92:95]\n\t"
      "v_mfma_f32_16x16x32_f16 v[92:95], v[196:199], v[232:235], v[92:95]\n\t"
      "v_mfma_f32_16x16x32_f16 v[92:95], v[200:203], v[228:231], v[92:95]\n\t"
      "v_mfma_f32_16x16x32_f16 v[92:95], v[204:207], v[224:227], v[92:95]\n\t"
      "s_branch CV2%=\n\t"
      "CV1%=:\n\t"
      "ds_read_b128 v[160:163], %[ub] offset:1792\n\t"
      "ds_read_b128 v[164:167], %[ub] offset:1536\n\t"
      "ds_read_b128 v[168:171], %[ub] offset:1280\n\t"
      "ds_read_b128 v[172:175], %[ub] offset:1024\n\t"
      "ds_read_b128 v[176:179], %[ub] offset:768\n\t"
      "ds_read_b128 v[180:183], %[ub] offset:512\n\t"
      "ds_read_b128 v[184:187], %[ub] offset:256\n\t"
      "ds_read_b128 v[188:191], %[ub] offset:0\n\t"
      "buffer_load_dwordx4 v[96:99], %[vs], %[rws], 0 offen sc1\n\t"
      "buffer_load_dwordx4 v[104:107], %[vs], %[rws], 0 offen offset:1024 sc1\n\t"
      "s_waitcnt lgkmcnt(7)\n\t"
      "v_mfma_f32_16x16x32_f16 a[240:243], v[128:131], v[160:163], 0\n\t"
      "ds_read_b128 v[224:227], %[ub] offset:1824\n\t"
      "s_waitcnt lgkmcnt(7)\n\t"
      "v_mfma_f32_16x16x32_f16 a[244:247], v[128:131], v[164:167], 0\n\t"
      "s_movk_i32 %[tmp], 0x1000\n\t"
      "ds_read2_b32 v[192:193], %[k0] offset0:16 offset1:17\n\t"
      "s_waitcnt lgkmcnt(7)\n\t"
      "v_mfma_f32_16x16x32_f16 a[248:251], v[128:131], v[168:171], 0\n\t"
      "buffer_load_dwordx4 v[100:103], %[vs], %[rws], %[tmp] offen sc1\n\t"
      "ds_read2_b32 v[194:195], %[k0] offset0:18 offset1:19\n\t"
      "s_waitcnt lgkmcnt(7)\n\t"
      "v_mfma_f32_16x16x32_f16 a[252:255], v[128:131], v[172:175], 0\n\t"
      "ds_read_b128 v[228:231], %[ub] offset:1568\n\t"
      "s_waitcnt lgkmcnt(7)\n\t"
      "v_mfma_f32_16x16x32_f16 v[80:83], v[128:131], v[176:179], 0\n\t"
      "buffer_load_dwordx4 v[108:111], %[vs], %[rws], %[tmp] offen offset:1024 sc1\n\t"
      "ds_read2_b32 v[196:197], %[k0] offset0:144 offset1:145\n\t"
      "s_waitcnt lgkmcnt(7)\n\t"
      "v_mfma_f32_16x16x32_f16 v[84:87], v[128:131], v[180:183], 0\n\t"
      "ds_read2_b32 v[198:199], %[k0] offset0:146 offset1:147\n\t"
      "s_waitcnt lgkmcnt(7)\n\t"
      "v_mfma_f32_16x16x32_f16 v[88:91], v[128:131], v[184:187], 0\n\t"
      "ds_read_b128 v[232:235], %[ub] offset:1312\n\t"
      "s_add_u32 m0, %[mA], 10240\n\t"
      "s_waitcnt lgkmcnt(7)\n\t"
      "v_mfma_f32_16x16x32_f16 v[92:95], v[128:131], v[188:191], 0\n\t"
      "ds_read2_b32 v[200:201], %[k1] offset0:16 offset1:17\n\t"
      "s_add_u32 %[tmp], %[sA], 286720\n\t"
      "v_mfma_f32_16x16x32_f16 a[244:247], v[132:135], v[160:163], a[244:247]\n\t"
      "ds_read2_b32 v[202:203], %[k1] offset0:18 offset1:19\n\t"
      "buffer_load_dwordx4 %[vA], %[rs], %[tmp] offen lds\n\t"
      "v_mfma_f32_16x16x32_f16 a[248:251], v[132:135], v[164:167], a[248:251]\n\t"
      "ds_read_b128 v[236:239], %[ub] offset:1056\n\t"
      "v_mfma_f32_16x16x32_f16 a[252:255], v[132:135], v[168:171], a[252:255]\n\t"
      "ds_read2_b32 v[204:205], %[k1] offset0:144 offset1:145\n\t"
      "s_add_u32 %[tw], %[sW], 0\n\t"
      "v_mfma_f32_16x16x32_f16 v[80:83], v[132:135], v[172:175], v[80:83]\n\t"
      "ds_read2_b32 v[206:207], %[k1] offset0:146 offset1:147\n\t"
      "buffer_load_dwordx4 a[16:19], %[w0], %[rws], %[tw] offen offset:0 sc1\n\t"
      "v_mfma_f32_16x16x32_f16 v[84:87], v[132:135], v[176:179], v[84:87]\n\t"
      "ds_read_b128 v[240:243], %[ub] offset:800\n\t"
      "v_mfma_f32_16x16x32_f16 v[88:91], v[132:135], v[180:183], v[88:91]\n\t"
      "ds_read2_b32 v[208:209], %[k2] offset0:16 offset1:17\n\t"
      "v_mfma_f32_16x16x32_f16 v[92:95], v[132:135], v[184:187], v[92:95]\n\t"
      "ds_read2_b32 v[210:211], %[k2] offset0:18 offset1:19\n\t"
      "v_mfma_f32_16x16x32_f16 a[248:251], v[136:139], v[160:163], a[248:251]\n\t"
      "ds_read_b128 v[244:247], %[ub] offset:544\n\t"
      "buffer_load_dwordx4 a[20:23], %[w1], %[rws], %[tw] offen offset:0 sc1\n\t"
      "v_mfma_f32_16x16x32_f16 a[252:255], v[136:139], v[164:167], a[252:255]\n\t"
      "ds_read2_b32 v[212:213], %[k2] offset0:144 offset1:145\n\t"
      "v_mfma_f32_16x16x32_f16 v[80:83], v[136:139], v[168:171], v[80:83]\n\t"
      "ds_read2_b32 v[214:215], %[k2] offset0:146 offset1:147\n\t"
      "v_mfma_f32_16x16x32_f16 v[84:87], v[136:139], v[172:175], v[84:87]\n\t"
      "ds_read_b128 v[248:251], %[ub] offset:288\n\t"
      "v_mfma_f32_16x16x32_f16 v[88:91], v[136:139], v[176:179], v[88:91]\n\t"
      "ds_read2_b32 v[216:217], %[k3] offset0:16 offset1:17\n\t"
      "buffer_load_dwordx4 a[24:27], %[w0], %[rws], %[tw] offen offset:2048 sc1\n\t"
      "v_mfma_f32_16x16x32_f16 v[92:95], v[136:139], v[180:183], v[92:95]\n\t"
      "ds_read2_b32 v[218:219], %[k3] offset0:18 offset1:19\n\t"
      "s_add_u32 m0, %[mA], 11264\n\t"
      "v_mfma_f32_16x16x32_f16 a[252:255], v[140:143], v[160:163], a[252:255]\n\t"
      "ds_read_b128 v[252:255], %[ub] offset:32\n\t"
      "s_add_u32 %[tmp], %[sA], 315392\n\t"
      "v_mfma_f32_16x16x32_f16 v[80:83], v[140:143], v[164:167], v[80:83]\n\t"
      "ds_read2_b32 v[220:221], %[k3] offset0:144 offset1:145\n\t"
      "buffer_load_dwordx4 %[vA], %[rs], %[tmp] offen lds\n\t"
      "v_mfma_f32_16x16x32_f16 v[84:87], v[140:143], v[168:171], v[84:87]\n\t"
      "ds_read2_b32 v[222:223], %[k3] offset0:146 offset1:147\n\t"
      "v_mfma_f32_16x16x32_f16 v[88:91], v[140:143], v[172:175], v[88:91]\n\t"
      "ds_read_b128 v[112:115], %[ua] offset:512\n\t"
      "v_mfma_f32_16x16x32_f16 v[92:95], v[140:143], v[176:179], v[92:95]\n\t"
      "ds_read_b128 v[116:119], %[ua]\n\t"
      "v_mfma_f32_16x16x32_f16 v[80:83], v[144:147], v[160:163], v[80:83]\n\t"
      "buffer_load_dwordx4 a[28:31], %[w1], %[rws], %[tw] offen offset:2048 sc1\n\t"
      "v_mfma_f32_16x16x32_f16 v[84:87], v[144:147], v[164:167], v[84:87]\n\t"
      "v_mfma_f32_16x16x32_f16 v[88:91], v[144:147], v[168:171], v[88:91]\n\t"
      "v_mfma_f32_16x16x32_f16 v[92:95], v[144:147], v[172:175], v[92:95]\n\t"
      "s_add_u32 %[tw], %[sW], 4096\n\t"
      "v_mfma_f32_16x16x32_f16 v[84:87], v[148:151], v[160:163], v[84:87]\n\t"
      "buffer_load_dwordx4 a[32:35], %[w0], %[rws], %[tw] offen offset:0 sc1\n\t"
      "v_mfma_f32_16x16x32_f16 v[88:91], v[148:151], v[164:167], v[88:91]\n\t"
      "s_add_u32 m0, %[mB], 0\n\t"
      "v_mfma_f32_16x16x32_f16 v[92:95], v[148:151], v[168:171], v[92:95]\n\t"
      "s_add_u32 %[tmp], %[sB], 0\n\t"
      "v_mfma_f32_16x16x32_f16 v[88:91], v[152:155], v[160:163], v[88:91]\n\t"
      "buffer_load_dwordx4 %[vl], %[rws], %[tmp] offen sc1 lds\n\t"
      "v_mfma_f32_16x16x32_f16 v[92:95], v[152:155], v[164:167], v[92:95]\n\t"
      "v_mfma_f32_16x16x32_f16 v[92:95], v[156:159], v[160:163], v[92:95]\n\t"
      "s_waitcnt lgkmcnt(0)\n\t"
      "v_mfma_f32_16x16x32_f16 a[240:243], v[192:195], v[224:227], a[240:243]\n\t"
      "ds_read_b128 v[160:163], %[ub] offset:1856\n\t"
      "v_mfma_f32_16x16x32_f16 a[244:247], v[192:195], v[228:231], a[244:247]\n\t"
      "ds_read2_b32 v[128:129], %[k0] offset0:32 offset1:33\n\t"
      "buffer_load_dwordx4 a[36:39], %[w1], %[rws], %[tw] offen offset:0 sc1\n\t"
      "v_mfma_f32_16x16x32_f16 a[248:251], v[192:195], v[232:235], a[248:251]\n\t"
      "ds_read2_b32 v[130:131], %[k0] offset0:34 offset1:35\n\t"
      "v_mfma_f32_16x16x32_f16 a[252:255], v[192:195], v[236:239], a[252:255]\n\t"
      "ds_read_b128 v[164:167], %[ub] offset:1600\n\t"
      "v_mfma_f32_16x16x32_f16 v[80:83], v[192:195], v[240:243], v[80:83]\n\t"
      "ds_read2_b32 v[132:133], %[k0] offset0:160 offset1:161\n\t"
      "v_mfma_f32_16x16x32_f16 v[84:87], v[192:195], v[244:247], v[84:87]\n\t"
      "ds_read2_b32 v[134:135], %[k0] offset0:162 offset1:163\n\t"
      "buffer_load_dwordx4 a[40:43], %[w0], %[rws], %[tw] offen offset:2048 sc1\n\t"
      "v_mfma_f32_16x16x32_f16 v[88:91], v[192:195], v[248:251], v[88:91]\n\t"
      "ds_read_b128 v[168:171], %[ub] offset:1344\n\t"
      "v_mfma_f32_16x16x32_f16 v[92:95], v[192:195], v[252:255], v[92:95]\n\t"
      "ds_read2_b32 v[136:137], %[k1] offset0:32 offset1:33\n\t"
      "v_mfma_f32_16x16x32_f16 a[244:247], v[196:199], v[224:227], a[244:247]\n\t"
      "ds_read2_b32 v[138:139], %[k1] offset0:34 offset1:35\n\t"
      "buffer_load_dwordx4 a[44:47], %[w1], %[rws], %[tw] offen offset:2048 sc1\n\t"
      "v_mfma_f32_16x16x32_f16 a[248:251], v[196:199], v[228:231], a[248:251]\n\t"
      "ds_read_b128 v[172:175], %[ub] offset:1088\n\t"
      "v_mfma_f32_16x16x32_f16 a[252:255], v[196:199], v[232:235], a[252:255]\n\t"
      "ds_read2_b32 v[140:141], %[k1] offset0:160 offset1:161\n\t"
      "s_add_u32 m0, %[mB], 1024\n\t"
      "v_mfma_f32_16x16x32_f16 v[80:83], v[196:199], v[236:239], v[80:83]\n\t"
      "ds_read2_b32 v[142:143], %[k1] offset0:162 offset1:163\n\t"
      "s_add_u32 %[tmp], %[sB], 1024\n\t"
      "v_mfma_f32_16x16x32_f16 v[84:87], v[196:199], v[240:243], v[84:87]\n\t"
      "ds_read_b128 v[176:179], %[ub] offset:832\n\t"
      "buffer_load_dwordx4 %[vl], %[rws], %[tmp] offen sc1 lds\n\t"
      "v_mfma_f32_16x16x32_f16 v[88:91], v[196:199], v[244:247], v[88:91]\n\t"
      "ds_read2_b32 v[144:145], %[k2] offset0:32 offset1:33\n\t"
      "v_mfma_f32_16x16x32_f16 v[92:95], v[196:199], v[248:251], v[92:95]\n\t"
      "ds_read2_b32 v[146:147], %[k2] offset0:34 offset1:35\n\t"
      "v_mfma_f32_16x16x32_f16 a[248:251], v[200:203], v[224:227], a[248:251]\n\t"
      "ds_read_b128 v[180:183], %[ub] offset:576\n\t"
      "s_add_u32 %[tw], %[sW], 8192\n\t"
      "v_mfma_f32_16x16x32_f16 a[252:255], v[200:203], v[228:231], a[252:255]\n\t"
      "ds_read2_b32 v[148:149], %[k2] offset0:160 offset1:161\n\t"
      "buffer_load_dwordx4 a[48:51], %[w0], %[rws], %[tw] offen offset:0 sc1\n\t"
      "v_mfma_f32_16x16x32_f16 v[80:83], v[200:203], v[232:235], v[80:83]\n\t"
      "ds_read2_b32 v[150:151], %[k2] offset0:162 offset1:163\n\t"
      "v_mfma_f32_16x16x32_f16 v[84:87], v[200:203], v[236:239], v[84:87]\n\t"
      "ds_read_b128 v[184:187], %[ub] offset:320\n\t"
      "v_mfma_f32_16x16x32_f16 v[88:91], v[200:203], v[240:243], v[88:91]\n\t"
      "ds_read2_b32 v[152:153], %[k3] offset0:32 offset1:33\n\t"
      "buffer_load_dwordx4 a[52:55], %[w1], %[rws], %[tw] offen offset:0 sc1\n\t"
      "v_mfma_f32_16x16x32_f16 v[92:95], v[200:203], v[244:247], v[92:95]\n\t"
      "ds_read2_b32 v[154:155], %[k3] offset0:34 offset1:35\n\t"
      "v_mfma_f32_16x16x32_f16 a[252:255], v[204:207], v[224:227], a[252:255]\n\t"
      "ds_read_b128 v[188:191], %[ub] offset:64\n\t"
      "s_add_u32 m0, %[mB], 2048\n\t"
      "v_mfma_f32_16x16x32_f16 v[80:83], v[204:207], v[228:231], v[80:83]\n\t"
      "ds_read2_b32 v[156:157], %[k3] offset0:160 offset1:161\n\t"
      "s_add_u32 %[tmp], %[sB], 2048\n\t"
      "v_mfma_f32_16x16x32_f16 v[84:87], v[204:207], v[232:235], v[84:87]\n\t"
      "ds_read2_b32 v[158:159], %[k3] offset0:162 offset1:163\n\t"
      "buffer_load_dwordx4 %[vl], %[rws], %[tmp] offen sc1 lds\n\t"
      "v_mfma_f32_16x16x32_f16 v[88:91], v[204:207], v[236:239], v[88:91]\n\t"
      "v_mfma_f32_16x16x32_f16 v[92:95], v[204:207], v[240:243], v[92:95]\n\t"
      "s_waitcnt vmcnt(15)\n\t"
      "v_mfma_f32_16x16x32_f16 v[80:83], v[208:211], v[224:227], v[80:83]\n\t"
      "v_and_b32 v120, 0xffff0000, v96\n\t"
      "v_lshlrev_b32 v96, 16, v96\n\t"
      "v_mfma_f32_16x16x32_f16 v[84:87], v[208:211], v[228:231], v[84:87]\n\t"
      "buffer_load_dwordx4 a[56:59], %[w0], %[rws], %[tw] offen offset:2048 sc1\n\t"
      "v_add_f32 v120, %[dk], v120\n\t"
      "v_fma_mix_f32 v96, v115, v120, v96 op_sel:[1,0,0] op_sel_hi:[1,0,0]\n\t"
      "v_mfma_f32_16x16x32_f16 v[88:91], v[208:211], v[232:235], v[88:91]\n\t"
      "v_and_b32 v120, 0xffff0000, v97\n\t"
      "v_lshlrev_b32 v97, 16, v97\n\t"
      "v_mfma_f32_16x16x32_f16 v[92:95], v[208:211], v[236:239], v[92:95]\n\t"
      "v_add_f32 v120, %[dk], v120\n\t"
      "v_fma_mix_f32 v97, v115, v120, v97 op_sel:[0,0,0] op_sel_hi:[1,0,0]\n\t"
      "v_mfma_f32_16x16x32_f16 v[84:87], v[212:215], v[224:227], v[84:87]\n\t"
      "buffer_load_dwordx4 a[60:63], %[w1], %[rws], %[tw] offen offset:2048 sc1\n\t"
      "v_and_b32 v120, 0xffff0000, v98\n\t"
      "v_lshlrev_b32 v98, 16, v98\n\t"
      "v_mfma_f32_16x16x32_f16 v[88:91], v[212:215], v[228:231], v[88:91]\n\t"
      "v_add_f32 v120, %[dk], v120\n\t"
      "v_fma_mix_f32 v98, v114, v120, v98 op_sel:[1,0,0] op_sel_hi:[1,0,0]\n\t"
      "v_mfma_f32_16x16x32_f16 v[92:95], v[212:215], v[232:235], v[92:95]\n\t"
      "s_add_u32 m0, %[mB], 3072\n\t"
      "v_and_b32 v120, 0xffff0000, v99\n\t"
      "v_lshlrev_b32 v99, 16, v99\n\t"
      "v_mfma_f32_16x16x32_f16 v[88:91], v[216:219], v[224:227], v[88:91]\n\t"
      "s_add_u32 %[tmp], %[sB], 3072\n\t"
      "v_add_f32 v120, %[dk], v120\n\t"
      "v_fma_mix_f32 v99, v114, v120, v99 op_sel:[0,0,0] op_sel_hi:[1,0,0]\n\t"
      "v_mfma_f32_16x16x32_f16 v[92:95], v[216:219], v[228:231], v[92:95]\n\t"
      "buffer_load_dwordx4 %[vl], %[rws], %[tmp] offen sc1 lds\n\t"
      "v_and_b32 v120, 0xffff0000, v100\n\t"
      "v_lshlrev_b32 v100, 16, v100\n\t"
      "v_mfma_f32_16x16x32_f16 v[92:95], v[220:223], v[224:227], v[92:95]\n\t"
      "v_add_f32 v120, %[dk], v120\n\t"
      "v_fma_mix_f32 v100, v113, v120, v100 op_sel:[1,0,0] op_sel_hi:[1,0,0]\n\t"
      "s_waitcnt lgkmcnt(0)\n\t"
      "v_mfma_f32_16x16x32_f16 a[240:243], v[128:131], v[160:163], a[240:243]\n\t"
      "ds_read_b128 v[224:227], %[ub] offset:1888\n\t"
      "v_mfma_f32_16x16x32_f16 a[244:247], v[128:131], v[164:167], a[244:247]\n\t"
      "ds_read2_b32 v[192:193], %[k0] offset0:48 offset1:49\n\t"
      "s_add_u32 %[tw], %[sW], 12288\n\t"
      "v_mfma_f32_16x16x32_f16 a[248:251], v[128:131], v[168:171], a[248:251]\n\t"
      "ds_read2_b32 v[194:195], %[k0] offset0:50 offset1:51\n\t"
      "buffer_load_dwordx4 a[64:67], %[w0], %[rws], %[tw] offen offset:0 sc1\n\t"
      "v_mfma_f32_16x16x32_f16 a[252:255], v[128:131], v[172:175], a[252:255]\n\t"
      "ds_read_b128 v[228:231], %[ub] offset:1632\n\t"
      "v_mfma_f32_16x16x32_f16 v[80:83], v[128:131], v[176:179], v[80:83]\n\t"
      "ds_read2_b32 v[196:197], %[k0] offset0:176 offset1:177\n\t"
      "v_mfma_f32_16x16x32_f16 v[84:87], v[128:131], v[180:183], v[84:87]\n\t"
      "ds_read2_b32 v[198:199], %[k0] offset0:178 offset1:179\n\t"
      "buffer_load_dwordx4 a[68:71], %[w1], %[rws], %[tw] offen offset:0 sc1\n\t"
      "v_mfma_f32_16x16x32_f16 v[88:91], v[128:131], v[184:187], v[88:91]\n\t"
      "ds_read_b128 v[232:235], %[ub] offset:1376\n\t"
      "v_mfma_f32_16x16x32_f16 v[92:95], v[128:131], v[188:191], v[92:95]\n\t"
      "ds_read2_b32 v[200:201], %[k1] offset0:48 offset1:49\n\t"
      "v_mfma_f32_16x16x32_f16 a[244:247], v[132:135], v[160:163], a[244:247]\n\t"
      "ds_read2_b32 v[202:203], %[k1] offset0:50 offset1:51\n\t"
      "v_mfma_f32_16x16x32_f16 a[248:251], v[132:135], v[164:167], a[248:251]\n\t"
      "ds_read_b128 v[236:239], %[ub] offset:1120\n\t"
      "buffer_load_dwordx4 a[72:75], %[w0], %[rws], %[tw] offen offset:2048 sc1\n\t"
      "v_mfma_f32_16x16x32_f16 a[252:255], v[132:135], v[168:171], a[252:255]\n\t"
      "ds_read2_b32 v[204:205], %[k1] offset0:176 offset1:177\n\t"
      "v_mfma_f32_16x16x32_f16 v[80:83], v[132:135], v[172:175], v[80:83]\n\t"
      "ds_read2_b32 v[206:207], %[k1] offset0:178 offset1:179\n\t"
      "s_add_u32 m0, %[mB], 4096\n\t"
      "v_mfma_f32_16x16x32_f16 v[84:87], v[132:135], v[176:179], v[84:87]\n\t"
      "ds_read_b128 v[240:243], %[ub] offset:864\n\t"
      "s_add_u32 %[tmp], %[sB], 4096\n\t"
      "v_mfma_f32_16x16x32_f16 v[88:91], v[132:135], v[180:183], v[88:91]\n\t"
      "ds_read2_b32 v[208:209], %[k2] offset0:48 offset1:49\n\t"
      "buffer_load_dwordx4 %[vl], %[rws], %[tmp] offen sc1 lds\n\t"
      "v_mfma_f32_16x16x32_f16 v[92:95], v[132:135], v[184:187], v[92:95]\n\t"
      "ds_read2_b32 v[210:211], %[k2] offset0:50 offset1:51\n\t"
      "v_mfma_f32_16x16x32_f16 a[248:251], v[136:139], v[160:163], a[248:251]\n\t"
      "ds_read_b128 v[244:247], %[ub] offset:608\n\t"
      "v_mfma_f32_16x16x32_f16 a[252:255], v[136:139], v[164:167], a[252:255]\n\t"
      "ds_read2_b32 v[212:213], %[k2] offset0:176 offset1:177\n\t"
      "buffer_load_dwordx4 a[76:79], %[w1], %[rws], %[tw] offen offset:2048 sc1\n\t"
      "v_mfma_f32_16x16x32_f16 v[80:83], v[136:139], v[168:171], v[80:83]\n\t"
      "ds_read2_b32 v[214:215], %[k2] offset0:178 offset1:179\n\t"
      "v_mfma_f32_16x16x32_f16 v[84:87], v[136:139], v[172:175], v[84:87]\n\t"
      "ds_read_b128 v[248:251], %[ub] offset:352\n\t"
      "v_mfma_f32_16x16x32_f16 v[88:91], v[136:139], v[176:179], v[88:91]\n\t"
      "ds_read2_b32 v[216:217], %[k3] offset0:48 offset1:49\n\t"
      "s_add_u32 %[tw], %[sW], 16384\n\t"
      "v_mfma_f32_16x16x32_f16 v[92:95], v[136:139], v[180:183], v[92:95]\n\t"
      "ds_read2_b32 v[218:219], %[k3] offset0:50 offset1:51\n\t"
      "buffer_load_dwordx4 a[80:83], %[w0], %[rws], %[tw] offen offset:0 sc1\n\t"
      "v_mfma_f32_16x16x32_f16 a[252:255], v[140:143], v[160:163], a[252:255]\n\t"
      "ds_read_b128 v[252:255], %[ub] offset:96\n\t"
      "v_mfma_f32_16x16x32_f16 v[80:83], v[140:143], v[164:167], v[80:83]\n\t"
      "ds_read2_b32 v[220:221], %[k3] offset0:176 offset1:177\n\t"
      "s_add_u32 m0, %[mB], 5120\n\t"
      "v_mfma_f32_16x16x32_f16 v[84:87], v[140:143], v[168:171], v[84:87]\n\t"
      "ds_read2_b32 v[222:223], %[k3] offset0:178 offset1:179\n\t"
      "s_add_u32 %[tmp], %[sB], 5120\n\t"
      "v_mfma_f32_16x16x32_f16 v[88:91], v[140:143], v[172:175], v[88:91]\n\t"
      "buffer_load_dwordx4 %[vl], %[rws], %[tmp] offen sc1 lds\n\t"
      "v_mfma_f32_16x16x32_f16 v[92:95], v[140:143], v[176:179], v[92:95]\n\t"
      "v_mfma_f32_16x16x32_f16 v[80:83], v[144:147], v[160:163], v[80:83]\n\t"
      "v_and_b32 v120, 0xffff0000, v101\n\t"
      "v_lshlrev_b32 v101, 16, v101\n\t"
      "v_mfma_f32_16x16x32_f16 v[84:87], v[144:147], v[164:167], v[84:87]\n\t"
      "buffer_load_dwordx4 a[84:87], %[w1], %[rws], %[tw] offen offset:0 sc1\n\t"
      "v_add_f32 v120, %[dk], v120\n\t"
      "v_fma_mix_f32 v101, v113, v120, v101 op_sel:[0,0,0] op_sel_hi:[1,0,0]\n\t"
      "v_mfma_f32_16x16x32_f16 v[88:91], v[144:147], v[168:171], v[88:91]\n\t"
      "v_and_b32 v120, 0xffff0000, v102\n\t"
      "v_lshlrev_b32 v102, 16, v102\n\t"
      "v_mfma_f32_16x16x32_f16 v[92:95], v[144:147], v[172:175], v[92:95]\n\t"
      "v_add_f32 v120, %[dk], v120\n\t"
      "v_fma_mix_f32 v102, v112, v120, v102 op_sel:[1,0,0] op_sel_hi:[1,0,0]\n\t"
      "v_mfma_f32_16x16x32_f16 v[84:87], v[148:151], v[160:163], v[84:87]\n\t"
      "v_and_b32 v120, 0xffff0000, v103\n\t"
      "v_lshlrev_b32 v103, 16, v103\n\t"
      "v_mfma_f32_16x16x32_f16 v[88:91], v[148:151], v[164:167], v[88:91]\n\t"
      "buffer_load_dwordx4 a[88:91], %[w0], %[rws], %[tw] offen offset:2048 sc1\n\t"
      "v_add_f32 v120, %[dk], v120\n\t"
      "v_fma_mix_f32 v103, v112, v120, v103 op_sel:[0,0,0] op_sel_hi:[1,0,0]\n\t"
      "v_mfma_f32_16x16x32_f16 v[92:95], v[148:151], v[168:171], v[92:95]\n\t"
      "v_and_b32 v120, 0xffff0000, v104\n\t"
      "v_lshlrev_b32 v104, 16, v104\n\t"
      "v_mfma_f32_16x16x32_f16 v[88:91], v[152:155], v[160:163], v[88:91]\n\t"
      "v_add_f32 v120, %[dk], v120\n\t"
      "v_fma_mix_f32 v104, v119, v120, v104 op_sel:[1,0,0] op_sel_hi:[1,0,0]\n\t"
      "v_mfma_f32_16x16x32_f16 v[92:95], v[152:155], v[164:167], v[92:95]\n\t"
      "v_and_b32 v120, 0xffff0000, v105\n\t"
      "v_lshlrev_b32 v105, 16, v105\n\t"
      "v_mfma_f32_16x16x32_f16 v[92:95], v[156:159], v[160:163], v[92:95]\n\t"
      "buffer_load_dwordx4 a[92:95], %[w1], %[rws], %[tw] offen offset:2048 sc1\n\t"
      "v_add_f32 v120, %[dk], v120\n\t"
      "v_fma_mix_f32 v105, v119, v120, v105 op_sel:[0,0,0] op_sel_hi:[1,0,0]\n\t"
      "s_waitcnt lgkmcnt(0)\n\t"
      "v_mfma_f32_16x16x32_f16 a[240:243], v[192:195], v[224:227], a[240:243]\n\t"
      "s_add_u32 m0, %[mB], 6144\n\t"
      "v_and_b32 v120, 0xffff0000, v106\n\t"
      "v_lshlrev_b32 v106, 16, v106\n\t"
      "v_mfma_f32_16x16x32_f16 a[244:247], v[192:195], v[228:231], a[244:247]\n\t"
      "s_add_u32 %[tmp], %[sB], 6144\n\t"
      "v_add_f32 v120, %[dk], v120\n\t"
      "v_fma_mix_f32 v106, v118, v120, v106 op_sel:[1,0,0] op_sel_hi:[1,0,0]\n\t"
      "v_mfma_f32_16x16x32_f16 a[244:247], v[196:199], v[224:227], a[244:247]\n\t"
      "buffer_load_dwordx4 %[vl], %[rws], %[tmp] offen sc1 lds\n\t"
      "v_and_b32 v120, 0xffff0000, v107\n\t"
      "v_lshlrev_b32 v107, 16, v107\n\t"
      "v_mfma_f32_16x16x32_f16 a[248:251], v[192:195], v[232:235], a[248:251]\n\t"
      "v_add_f32 v120, %[dk], v120\n\t"
      "v_fma_mix_f32 v107, v118, v120, v107 op_sel:[0,0,0] op_sel_hi:[1,0,0]\n\t"
      "v_mfma_f32_16x16x32_f16 a[248:251], v[196:199], v[228:231], a[248:251]\n\t"
      "v_and_b32 v120, 0xffff0000, v108\n\t"
      "v_lshlrev_b32 v108, 16, v108\n\t"
      "v_mfma_f32_16x16x32_f16 a[248:251], v[200:203], v[224:227], a[248:251]\n\t"
      "s_add_u32 %[tw], %[sW], 20480\n\t"
      "v_add_f32 v120, %[dk], v120\n\t"
      "v_fma_mix_f32 v108, v117, v120, v108 op_sel:[1,0,0] op_sel_hi:[1,0,0]\n\t"
      "v_mfma_f32_16x16x32_f16 a[252:255], v[192:195], v[236:239], a[252:255]\n\t"
      "buffer_load_dwordx4 a[96:99], %[w0], %[rws], %[tw] offen offset:0 sc1\n\t"
      "v_and_b32 v120, 0xffff0000, v109\n\t"
      "v_lshlrev_b32 v109, 16, v109\n\t"
      "v_mfma_f32_16x16x32_f16 a[252:255], v[196:199], v[232:235], a[252:255]\n\t"
      "v_add_f32 v120, %[dk], v120\n\t"
      "v_fma_mix_f32 v109, v117, v120, v109 op_sel:[0,0,0] op_sel_hi:[1,0,0]\n\t"
      "v_mfma_f32_16x16x32_f16 a[252:255], v[200:203], v[228:231], a[252:255]\n\t"
      "v_and_b32 v120, 0xffff0000, v110\n\t"
      "v_lshlrev_b32 v110, 16, v110\n\t"
      "v_mfma_f32_16x16x32_f16 a[252:255], v[204:207], v[224:227], a[252:255]\n\t"
      "v_add_f32 v120, %[dk], v120\n\t"
      "v_fma_mix_f32 v110, v116, v120, v110 op_sel:[1,0,0] op_sel_hi:[1,0,0]\n\t"
      "v_mfma_f32_16x16x32_f16 v[80:83], v[192:195], v[240:243], v[80:83]\n\t"
      "buffer_load_dwordx4 a[100:103], %[w1], %[rws], %[tw] offen offset:0 sc1\n\t"
      "v_and_b32 v120, 0xffff0000, v111\n\t"
      "v_lshlrev_b32 v111, 16, v111\n\t"
      "v_mfma_f32_16x16x32_f16 v[80:83], v[196:199], v[236:239], v[80:83]\n\t"
      "s_add_u32 m0, %[mB], 7168\n\t"
      "v_add_f32 v120, %[dk], v120\n\t"
      "v_fma_mix_f32 v111, v116, v120, v111 op_sel:[0,0,0] op_sel_hi:[1,0,0]\n\t"
      "v_mfma_f32_16x16x32_f16 v[80:83], v[200:203], v[232:235], v[80:83]\n\t"
      "s_add_u32 %[tmp], %[sB], 7168\n\t"
      "v_mfma_f32_16x16x32_f16 v[80:83], v[204:207], v[228:231], v[80:83]\n\t"
      "buffer_load_dwordx4 %[vl], %[rws], %[tmp] offen sc1 lds\n\t"
      "ds_write_b128 %[pr], a[240:243] offset:0\n\t"
      "v_mfma_f32_16x16x32_f16 v[80:83], v[208:211], v[224:227], v[80:83]\n\t"
      "v_mfma_f32_16x16x32_f16 v[84:87], v[192:195], v[244:247], v[84:87]\n\t"
      "v_mfma_f32_16x16x32_f16 v[84:87], v[196:199], v[240:243], v[84:87]\n\t"
      "ds_write_b128 %[pr], a[244:247] offset:1024\n\t"
      "v_mfma_f32_16x16x32_f16 v[84:87], v[200:203], v[236:239], v[84:87]\n\t"
      "buffer_load_dwordx4 a[104:107], %[w0], %[rws], %[tw] offen offset:2048 sc1\n\t"
      "v_mfma_f32_16x16x32_f16 v[84:87], v[204:207], v[232:235], v[84:87]\n\t"
      "v_mfma_f32_16x16x32_f16 v[84:87], v[208:211], v[228:231], v[84:87]\n\t"
      "ds_write_b128 %[pr], a[248:251] offset:2048\n\t"
      "v_mfma_f32_16x16x32_f16 v[84:87], v[212:215], v[224:227], v[84:87]\n\t"
      "v_mfma_f32_16x16x32_f16 v[88:91], v[192:195], v[248:251], v[88:91]\n\t"
      "buffer_load_dwordx4 a[108:111], %[w1], %[rws], %[tw] offen offset:2048 sc1\n\t"
      "v_mfma_f32_16x16x32_f16 v[88:91], v[196:199], v[244:247], v[88:91]\n\t"
      "ds_write_b128 %[pr], a[252:255] offset:3072\n\t"
      "v_mfma_f32_16x16x32_f16 v[88:91], v[200:203], v[240:243], v[88:91]\n\t"
      "v_mfma_f32_16x16x32_f16 v[88:91], v[204:207], v[236:239], v[88:91]\n\t"
      "v_mfma_f32_16x16x32_f16 v[88:91], v[208:211], v[232:235], v[88:91]\n\t"
      "v_mfma_f32_16x16x32_f16 v[88:91], v[212:215], v[228:231], v[88:91]\n\t"
      "v_mfma_f32_16x16x32_f16 v[88:91], v[216:219], v[224:227], v[88:91]\n\t"
      "v_mfma_f32_16x16x32_f16 v[92:95], v[192:195], v[252:255], v[92:95]\n\t"
      "v_mfma_f32_16x16x32_f16 v[92:95], v[196:199], v[248:251], v[92:95]\n\t"
      "v_mfma_f32_16x16x32_f16 v[92:95], v[200:203], v[244:247], v[92:95]\n\t"
      "v_mfma_f32_16x16x32_f16 v[92:95], v[204:207], v[240:243], v[92:95]\n\t"
      "v_mfma_f32_16x16x32_f16 v[92:95], v[208:211], v[236:239], v[92:95]\n\t"
      "v_mfma_f32_16x16x32_f16 v[92:95], v[212:215], v[232:235], v[92:95]\n\t"
      "v_mfma_f32_16x16x32_f16 v[92:95], v[216:219], v[228:231], v[92:95]\n\t"
      "v_mfma_f32_16x16x32_f16 v[92:95], v[220:223], v[224:227], v[92:95]\n\t"
      "CV2%=:\n\t"
      "s_waitcnt lgkmcnt(0)\n\t"
      "s_barrier\n\t"
      "ds_read_b128 v[128:131], %[pq] offset:0\n\t"
      "ds_read_b128 v[132:135], %[pq] offset:1024\n\t"
      "ds_read_b128 v[136:139], %[pq] offset:2048\n\t"
      "ds_read_b128 v[140:143], %[pq] offset:3072\n\t"
      "s_nop 1\n\t"
      "s_waitcnt lgkmcnt(2)\n\t"
      "v_pk_add_f32 v[80:81], v[80:81], v[128:129]\n\t"
      "v_pk_add_f32 v[82:83], v[82:83], v[130:131]\n\t"
      "v_pk_add_f32 v[84:85], v[84:85], v[132:133]\n\t"
      "v_pk_add_f32 v[86:87], v[86:87], v[134:135]\n\t"
      "s_nop 0\n\t"
      "v_permlane16_swap_b32 v80, v84\n\t"
      "v_permlane16_swap_b32 v81, v85\n\t"
      "v_permlane16_swap_b32 v82, v86\n\t"
      "v_permlane16_swap_b32 v83, v87\n\t"
      "v_pk_fma_f32 v[80:81], v[80:81], %[isc], v[96:97]\n\t"
      "v_pk_fma_f32 v[82:83], v[82:83], %[isc], v[98:99]\n\t"
      "v_pk_fma_f32 v[84:85], v[84:85], %[isc], v[100:101]\n\t"
      "v_pk_fma_f32 v[86:87], v[86:87], %[isc], v[102:103]\n\t"
      "v_cvt_pk_bf16_f32 v112, v80, v81\n\t"
      "v_cvt_pk_bf16_f32 v113, v82, v83\n\t"
      "v_cvt_pk_bf16_f32 v114, v84, v85\n\t"
      "v_cvt_pk_bf16_f32 v115, v86, v87\n\t"
      "buffer_store_dwordx4 v[112:115], %[vh], %[rws], 0 offen sc1\n\t"
      "s_mov_b32 %[tmp], 0x80000\n\t"
      "s_waitcnt lgkmcnt(0)\n\t"
      "v_pk_add_f32 v[88:89], v[88:89], v[136:137]\n\t"
      "v_pk_add_f32 v[90:91], v[90:91], v[138:139]\n\t"
      "v_pk_add_f32 v[92:93], v[92:93], v[140:141]\n\t"
      "v_pk_add_f32 v[94:95], v[94:95], v[142:143]\n\t"
      "s_nop 0\n\t"
      "v_permlane16_swap_b32 v88, v92\n\t"
      "v_permlane16_swap_b32 v89, v93\n\t"
      "v_permlane16_swap_b32 v90, v94\n\t"
      "v_permlane16_swap_b32 v91, v95\n\t"
      "v_pk_fma_f32 v[88:89], v[88:89], %[isc], v[104:105]\n\t"
      "v_pk_fma_f32 v[90:91], v[90:91], %[isc], v[106:107]\n\t"
      "v_pk_fma_f32 v[92:93], v[92:93], %[isc], v[108:109]\n\t"
      "v_pk_fma_f32 v[94:95], v[94:95], %[isc], v[110:111]\n\t"
      "v_cvt_pk_bf16_f32 v116, v88, v89\n\t"
      "v_cvt_pk_bf16_f32 v117, v90, v91\n\t"
      "v_cvt_pk_bf16_f32 v118, v92, v93\n\t"
      "v_cvt_pk_bf16_f32 v119, v94, v95\n\t"
      "buffer_store_dwordx4 v[116:119], %[vh], %[rws], %[tmp] offen sc1\n\t"
      "s_nop 1\n\t"
      "s_add_u32 %[tw], %[sW], 24576\n\t"
      "buffer_load_dwordx4 a[112:115], %[w0], %[rws], %[tw] offen offset:0 sc1\n\t"
      "buffer_load_dwordx4 a[116:119], %[w1], %[rws], %[tw] offen offset:0 sc1\n\t"
      "buffer_load_dwordx4 a[120:123], %[w0], %[rws], %[tw] offen offset:2048 sc1\n\t"
      "buffer_load_dwordx4 a[124:127], %[w1], %[rws], %[tw] offen offset:2048 sc1\n\t"
      "s_add_u32 %[tw], %[sW], 28672\n\t"
      "buffer_load_dwordx4 a[128:131], %[w0], %[rws], %[tw] offen offset:0 sc1\n\t"
      "buffer_load_dwordx4 a[132:135], %[w1], %[rws], %[tw] offen offset:0 sc1\n\t"
      "buffer_load_dwordx4 a[136:139], %[w0], %[rws], %[tw] offen offset:2048 sc1\n\t"
      "buffer_load_dwordx4 a[140:143], %[w1], %[rws], %[tw] offen offset:2048 sc1\n\t"
        : [tmp] "=&s"(tmp), [tw] "=&s"(tw)
        : [ub] "v"(L_UI + cw * UI_CH + (g4 & 1) * UI_H + (15 - r16 + (g4 >> 1) + 8 * sh) * 16), [k0] "v"(kq), [k1] "v"(kq + 1024), [k2] "v"(kq + 2048),
          [k3] "v"(kq + 3072),
          [pr] "v"(L_W1B + w * 12288 + 8192 + lane * 16), [pq] "v"(L_W1B + (w ^ 1) * 12288 + 8192 + lane * 16), [vA] "v"(vA), [vl] "v"(lane * 16), [rs] "s"(rw1), [mA] "s"(stA),
          [mB] "s"(L_W1B + w * 12288), [sA] "s"(sA), [sB] "s"(WS_PB + mem * 49152 + w * 12288), [rws] "s"(rws), [vs] "v"(WS_SD + tb0 * 16),
          [ua] "v"(L_UI + cw * UI_CH + (~tb0 & 1) * UI_H + ((191 - tb0) >> 1) * 16), [dk] "s"(RFL(BC(unsigned, dks))), [isc] "v"(f2{isc, isc}), [sh] "s"(sh), [sW] "s"(sW), [sV] "s"(sV), [w0] "v"(vW), [w1] "v"(vW ^ 1024u),
          [vh] "v"(WS_HT + (tb0 >> 2) * 32768 + d * 64 + (((tb0 & 3) ^ ((d >> 1) & 2)) * 16))
        : "memory", "scc");
  }
  VMWAIT(8);
  BAR();
  if (w == 0 && lane < 8) __hip_atomic_store((GAS u64*)(ws + WS_FB + lane * 2048 + b * 8), nonce, RLX);
  const unsigned aA = L_W1A + (4 * g4 + q4) * 128 + (2 * (g4 & 1) + (q4 >> 1)) * 32 + 8 * p4;
  const unsigned aB = L_W1B + (4 * g4 + q4) * 96 + 8 * p4;
  {
    unsigned tmp, tw;
    asm volatile(
      "s_add_u32 m0, %[mB], 8192\n\t"
      "s_add_u32 %[tmp], %[sB], 8192\n\t"
      "buffer_load_dwordx4 %[vl], %[rws], %[tmp] offen sc1 lds\n\t"
      "ds_read_b64_tr_b16 v[128:129], %[a0] offset:0\n\t"
      "ds_read_b64_tr_b16 v[130:131], %[a0] offset:2048\n\t"
      "ds_read_b64_tr_b16 v[132:133], %[a1] offset:0\n\t"
      "ds_read_b64_tr_b16 v[134:135], %[a1] offset:2048\n\t"
      "s_add_u32 m0, %[mB], 9216\n\t"
      "s_add_u32 %[tmp], %[sB], 9216\n\t"
      "buffer_load_dwordx4 %[vl], %[rws], %[tmp] offen sc1 lds\n\t"
      "ds_read_b64_tr_b16 v[136:137], %[a2] offset:0\n\t"
      "ds_read_b64_tr_b16 v[138:139], %[a2] offset:2048\n\t"
      "ds_read_b64_tr_b16 v[140:141], %[a3] offset:0\n\t"
      "ds_read_b64_tr_b16 v[142:143], %[a3] offset:2048\n\t"
      "s_add_u32 m0, %[mB], 10240\n\t"
      "s_add_u32 %[tmp], %[sB], 10240\n\t"
      "buffer_load_dwordx4 %[vl], %[rws], %[tmp] offen sc1 lds\n\t"
      "ds_read_b64_tr_b16 v[156:157], %[a0] offset:4096\n\t"
      "ds_read_b64_tr_b16 v[158:159], %[a0] offset:6144\n\t"
      "ds_read_b64_tr_b16 v[160:161], %[a1] offset:4096\n\t"
      "ds_read_b64_tr_b16 v[162:163], %[a1] offset:6144\n\t"
      "s_add_u32 m0, %[mB], 11264\n\t"
      "s_add_u32 %[tmp], %[sB], 11264\n\t"
      "buffer_load_dwordx4 %[vl], %[rws], %[tmp] offen sc1 lds\n\t"
      "ds_read_b64_tr_b16 v[164:165], %[a2] offset:4096\n\t"
      "ds_read_b64_tr_b16 v[166:167], %[a2] offset:6144\n\t"
      "ds_read_b64_tr_b16 v[168:169], %[a3] offset:4096\n\t"
      "ds_read_b64_tr_b16 v[170:171], %[a3] offset:6144\n\t"
      "s_add_u32 %[tw], %[sV], 0\n\t"
      "buffer_load_dwordx4 a[144:147], %[w0], %[rws], %[tw] offen offset:0 sc1\n\t"
      "ds_read_b64_tr_b16 v[144:145], %[ab] offset:0\n\t"
      "ds_read_b64_tr_b16 v[146:147], %[ab] offset:1536\n\t"
      "ds_read_b64_tr_b16 v[148:149], %[ab] offset:32\n\t"
      "ds_read_b64_tr_b16 v[150:151], %[ab] offset:1568\n\t"
      "ds_read_b64_tr_b16 v[152:153], %[ab] offset:64\n\t"
      "ds_read_b64_tr_b16 v[154:155], %[ab] offset:1600\n\t"
      "buffer_load_dwordx4 a[148:151], %[w1], %[rws], %[tw] offen offset:0 sc1\n\t"
      "ds_read_b64_tr_b16 v[172:173], %[ab] offset:3072\n\t"
      "ds_read_b64_tr_b16 v[174:175], %[ab] offset:4608\n\t"
      "ds_read_b64_tr_b16 v[176:177], %[ab] offset:3104\n\t"
      "ds_read_b64_tr_b16 v[178:179], %[ab] offset:4640\n\t"
      "ds_read_b64_tr_b16 v[180:181], %[ab] offset:3136\n\t"
      "ds_read_b64_tr_b16 v[182:183], %[ab] offset:4672\n\t"
      "buffer_load_dwordx4 a[152:155], %[w0], %[rws], %[tw] offen offset:2048 sc1\n\t"
      "buffer_load_dwordx4 a[156:159], %[w1], %[rws], %[tw] offen offset:2048 sc1\n\t"
      "s_add_u32 %[tw], %[sV], 4096\n\t"
      "buffer_load_dwordx4 a[160:163], %[w0], %[rws], %[tw] offen offset:0 sc1\n\t"
      "buffer_load_dwordx4 a[164:167], %[w1], %[rws], %[tw] offen offset:0 sc1\n\t"
      "buffer_load_dwordx4 a[168:171], %[w0], %[rws], %[tw] offen offset:2048 sc1\n\t"
      "buffer_load_dwordx4 a[172:175], %[w1], %[rws], %[tw] offen offset:2048 sc1\n\t"
      "s_waitcnt lgkmcnt(0)\n\t"
      "v_mfma_f32_16x16x32_bf16 v[108:111], v[128:131], v[48:51], 0\n\t"
      "ds_read_b64_tr_b16 v[184:185], %[a0] offset:8192\n\t"
      "ds_read_b64_tr_b16 v[186:187], %[a0] offset:10240\n\t"
      "v_mfma_f32_16x16x32_bf16 v[108:111], v[132:135], v[52:55], v[108:111]\n\t"
      "ds_read_b64_tr_b16 v[188:189], %[a1] offset:8192\n\t"
      "ds_read_b64_tr_b16 v[190:191], %[a1] offset:10240\n\t"
      "v_mfma_f32_16x16x32_bf16 v[108:111], v[136:139], v[56:59], v[108:111]\n\t"
      "ds_read_b64_tr_b16 v[192:193], %[a2] offset:8192\n\t"
      "ds_read_b64_tr_b16 v[194:195], %[a2] offset:10240\n\t"
      "v_mfma_f32_16x16x32_bf16 v[108:111], v[140:143], v[60:63], v[108:111]\n\t"
      "ds_read_b64_tr_b16 v[196:197], %[a3] offset:8192\n\t"
      "ds_read_b64_tr_b16 v[198:199], %[a3] offset:10240\n\t"
      "v_mfma_f32_16x16x32_bf16 v[108:111], v[144:147], v[64:67], v[108:111]\n\t"
      "ds_read_b64_tr_b16 v[212:213], %[a0] offset:12288\n\t"
      "ds_read_b64_tr_b16 v[214:215], %[a0] offset:14336\n\t"
      "v_mfma_f32_16x16x32_bf16 v[108:111], v[148:151], v[68:71], v[108:111]\n\t"
      "ds_read_b64_tr_b16 v[216:217], %[a1] offset:12288\n\t"
      "ds_read_b64_tr_b16 v[218:219], %[a1] offset:14336\n\t"
      "v_mfma_f32_16x16x32_bf16 v[108:111], v[152:155], v[72:75], v[108:111]\n\t"
      "ds_read_b64_tr_b16 v[220:221], %[a2] offset:12288\n\t"
      "ds_read_b64_tr_b16 v[222:223], %[a2] offset:14336\n\t"
      "v_mfma_f32_16x16x32_bf16 v[108:111], v[156:159], v[48:51], v[108:111]\n\t"
      "ds_read_b64_tr_b16 v[224:225], %[a3] offset:12288\n\t"
      "ds_read_b64_tr_b16 v[226:227], %[a3] offset:14336\n\t"
      "v_mfma_f32_16x16x32_bf16 v[108:111], v[160:163], v[52:55], v[108:111]\n\t"
      "s_waitcnt vmcnt(8)\n\t"
      "ds_read_b64_tr_b16 v[200:201], %[ab] offset:6144\n\t"
      "ds_read_b64_tr_b16 v[202:203], %[ab] offset:7680\n\t"
      "v_mfma_f32_16x16x32_bf16 v[108:111], v[164:167], v[56:59], v[108:111]\n\t"
      "ds_read_b64_tr_b16 v[204:205], %[ab] offset:6176\n\t"
      "ds_read_b64_tr_b16 v[206:207], %[ab] offset:7712\n\t"
      "v_mfma_f32_16x16x32_bf16 v[108:111], v[168:171], v[60:63], v[108:111]\n\t"
      "ds_read_b64_tr_b16 v[208:209], %[ab] offset:6208\n\t"
      "ds_read_b64_tr_b16 v[210:211], %[ab] offset:7744\n\t"
      "v_mfma_f32_16x16x32_bf16 v[108:111], v[172:175], v[64:67], v[108:111]\n\t"
      "ds_read_b64_tr_b16 v[228:229], %[ab] offset:9216\n\t"
      "ds_read_b64_tr_b16 v[230:231], %[ab] offset:10752\n\t"
      "v_mfma_f32_16x16x32_bf16 v[108:111], v[176:179], v[68:71], v[108:111]\n\t"
      "ds_read_b64_tr_b16 v[232:233], %[ab] offset:9248\n\t"
      "ds_read_b64_tr_b16 v[234:235], %[ab] offset:10784\n\t"
      "v_mfma_f32_16x16x32_bf16 v[108:111], v[180:183], v[72:75], v[108:111]\n\t"
      "ds_read_b64_tr_b16 v[236:237], %[ab] offset:9280\n\t"
      "ds_read_b64_tr_b16 v[238:239], %[ab] offset:10816\n\t"
        : [tmp] "=&s"(tmp), [tw] "=&s"(tw)
        : [a0] "v"(aA + w * 16384), [a1] "v"((aA ^ 32u) + w * 16384), [a2] "v"((aA ^ 64u) + w * 16384), [a3] "v"((aA ^ 96u) + w * 16384),
          [ab] "v"(aB + w * 12288), [rws] "s"(rws), [sW] "s"(sW), [sV] "s"(sV), [w0] "v"(vW), [w1] "v"(vW ^ 1024u),
          [mB] "s"(L_W1B + w * 12288), [sB] "s"(WS_PB + mem * 49152 + w * 12288), [vl] "v"(lane * 16)
        : "memory", "scc");
    if (w == 3) poll_sample(rws, vfa + (WS_FB - WS_FA));
    asm volatile(
      "s_waitcnt lgkmcnt(12)\n\t"
      "v_mfma_f32_16x16x32_bf16 v[108:111], v[184:187], v[48:51], v[108:111]\n\t"
      "v_mfma_f32_16x16x32_bf16 v[108:111], v[188:191], v[52:55], v[108:111]\n\t"
      "v_mfma_f32_16x16x32_bf16 v[108:111], v[192:195], v[56:59], v[108:111]\n\t"
      "v_mfma_f32_16x16x32_bf16 v[108:111], v[196:199], v[60:63], v[108:111]\n\t"
      "v_mfma_f32_16x16x32_bf16 v[108:111], v[212:215], v[48:51], v[108:111]\n\t"
      "v_mfma_f32_16x16x32_bf16 v[108:111], v[216:219], v[52:55], v[108:111]\n\t"
      "v_mfma_f32_16x16x32_bf16 v[108:111], v[220:223], v[56:59], v[108:111]\n\t"
      "v_mfma_f32_16x16x32_bf16 v[108:111], v[224:227], v[60:63], v[108:111]\n\t"
      "s_waitcnt lgkmcnt(0)\n\t"
      "v_mfma_f32_16x16x32_bf16 v[108:111], v[200:203], v[64:67], v[108:111]\n\t"
      "v_mfma_f32_16x16x32_bf16 v[108:111], v[204:207], v[68:71], v[108:111]\n\t"
      "v_mfma_f32_16x16x32_bf16 v[108:111], v[208:211], v[72:75], v[108:111]\n\t"
      "v_mfma_f32_16x16x32_bf16 v[108:111], v[228:231], v[64:67], v[108:111]\n\t"
      "v_mfma_f32_16x16x32_bf16 v[108:111], v[232:235], v[68:71], v[108:111]\n\t"
      "v_mfma_f32_16x16x32_bf16 v[108:111], v[236:239], v[72:75], v[108:111]\n\t"
      "s_nop 7\n\t"
      "s_mov_b32 exec_lo, 0x007f007f\n\t"
      "s_mov_b32 exec_hi, 0x007f007f\n\t"
      "ds_write_b128 %[dst], v[108:111]\n\t"
      "s_mov_b64 exec, -1\n\t"
        :: [dst] "v"(ring + 11264 + r16 * 64 + g4 * 16) : "memory");
  }
  if (w == 3) poll_finish(rws, vfa + (WS_FB - WS_FA), nlo, nhi);
  BAR();
  const unsigned sH = WS_HT + (4 * grp + w) * 32768, mR = ring;
  {
    unsigned tmp;
    asm volatile(
      "s_add_u32 m0, %[mR], 0\n\t"
      "s_add_u32 %[tmp], %[sH], 0\n\t"
      "buffer_load_dwordx4 %[vl], %[rs], %[tmp] offen offset:0 sc1 lds\n\t"
      "ds_read_b64_tr_b16 v[80:81], %[a0] offset:0\n\t"
      "buffer_load_dwordx4 %[vl], %[rs], %[tmp] offen offset:1024 sc1 lds\n\t"
      "ds_read_b64_tr_b16 v[82:83], %[a0] offset:2048\n\t"
      "buffer_load_dwordx4 %[vl], %[rs], %[tmp] offen offset:2048 sc1 lds\n\t"
      "ds_read_b64_tr_b16 v[84:85], %[a1] offset:0\n\t"
      "buffer_load_dwordx4 %[vl], %[rs], %[tmp] offen offset:3072 sc1 lds\n\t"
      "ds_read_b64_tr_b16 v[86:87], %[a1] offset:2048\n\t"
      "s_add_u32 m0, %[mR], 4096\n\t"
      "s_add_u32 %[tmp], %[sH], 4096\n\t"
      "buffer_load_dwordx4 %[vl], %[rs], %[tmp] offen offset:0 sc1 lds\n\t"
      "ds_read_b64_tr_b16 v[88:89], %[a2] offset:0\n\t"
      "buffer_load_dwordx4 %[vl], %[rs], %[tmp] offen offset:1024 sc1 lds\n\t"
      "ds_read_b64_tr_b16 v[90:91], %[a2] offset:2048\n\t"
      "buffer_load_dwordx4 %[vl], %[rs], %[tmp] offen offset:2048 sc1 lds\n\t"
      "ds_read_b64_tr_b16 v[92:93], %[a3] offset:0\n\t"
      "buffer_load_dwordx4 %[vl], %[rs], %[tmp] offen offset:3072 sc1 lds\n\t"
      "ds_read_b64_tr_b16 v[94:95], %[a3] offset:2048\n\t"
      "s_add_u32 m0, %[mR], 8192\n\t"
      "s_add_u32 %[tmp], %[sH], 8192\n\t"
      "buffer_load_dwordx4 %[vl], %[rs], %[tmp] offen offset:0 sc1 lds\n\t"
      "buffer_load_dwordx4 %[vl], %[rs], %[tmp] offen offset:1024 sc1 lds\n\t"
      "buffer_load_dwordx4 %[vl], %[rs], %[tmp] offen offset:2048 sc1 lds\n\t"
        : [tmp] "=&s"(tmp)
        : [mR] "s"(mR), [sH] "s"(sH), [vl] "v"(lane * 16), [rs] "s"(rws),
          [a0] "v"(aA), [a1] "v"(aA ^ 32u), [a2] "v"(aA ^ 64u), [a3] "v"(aA ^ 96u)
        : "memory", "scc");
  }
  const unsigned csp = ring + 11712 + g4 * 16;
  {
    unsigned tmp, tw;
    const unsigned hq = ring + (4 * g4 + q4) * 64 + (g4 & 1) * 32 + 8 * p4;
    asm volatile(
      "ds_read_b128 v[96:99], %[cp] offset:0\n\t"
      "ds_read_b128 v[100:103], %[cp] offset:12288\n\t"
      "ds_read_b128 v[104:107], %[cp] offset:24576\n\t"
      "ds_read_b128 v[108:111], %[cp] offset:36864\n\t"
      "s_waitcnt lgkmcnt(0)\n\t"
      "v_pk_add_f32 v[96:97], v[96:97], v[100:101]\n\t"
      "v_pk_add_f32 v[98:99], v[98:99], v[102:103]\n\t"
      "v_pk_add_f32 v[104:105], v[104:105], v[108:109]\n\t"
      "v_pk_add_f32 v[106:107], v[106:107], v[110:111]\n\t"
      "v_pk_add_f32 v[96:97], v[96:97], v[104:105]\n\t"
      "v_pk_add_f32 v[98:99], v[98:99], v[106:107]\n\t"
      "s_mov_b32 exec_lo, 0x0fffffff\n\t"
      "s_mov_b32 exec_hi, 0\n\t"
      "ds_write_b128 %[cq], v[96:99]\n\t"
      "s_mov_b64 exec, -1\n\t"
      "s_waitcnt vmcnt(9)\n\t"
      "ds_read_b64_tr_b16 v[128:129], %[h0] offset:0\n\t"
      "ds_read_b64_tr_b16 v[130:131], %[h0] offset:1024\n\t"
      "ds_read_b64_tr_b16 v[132:133], %[h1] offset:0\n\t"
      "ds_read_b64_tr_b16 v[134:135], %[h1] offset:1024\n\t"
      "s_waitcnt lgkmcnt(2)\n\t"
      "v_mfma_f32_16x16x32_bf16 a[240:243], v[124:127], v[128:131], 0\n\t"
      "s_waitcnt lgkmcnt(0)\n\t"
      "s_add_u32 m0, %[mR], 0\n\t"
      "s_add_u32 %[tmp], %[sH], 11264\n\t"
      "buffer_load_dwordx4 %[vl], %[rs], %[tmp] offen offset:0 sc1 lds\n\t"
      "v_mfma_f32_16x16x32_bf16 a[244:247], v[124:127], v[132:135], 0\n\t"
      "ds_read_b64_tr_b16 v[96:97], %[a0] offset:4096\n\t"
      "ds_read_b64_tr_b16 v[98:99], %[a0] offset:6144\n\t"
      "v_mfma_f32_16x16x32_bf16 v[48:51], v[80:83], v[128:131], 0\n\t"
      "ds_read_b64_tr_b16 v[100:101], %[a1] offset:4096\n\t"
      "v_mfma_f32_16x16x32_bf16 v[52:55], v[80:83], v[132:135], 0\n\t"
      "s_waitcnt vmcnt(8)\n\t"
      "ds_read_b64_tr_b16 v[102:103], %[a1] offset:6144\n\t"
      "v_mfma_f32_16x16x32_bf16 v[56:59], v[84:87], v[128:131], 0\n\t"
      "ds_read_b64_tr_b16 v[136:137], %[h0] offset:2048\n\t"
      "ds_read_b64_tr_b16 v[138:139], %[h0] offset:3072\n\t"
      "v_mfma_f32_16x16x32_bf16 v[60:63], v[84:87], v[132:135], 0\n\t"
      "ds_read_b64_tr_b16 v[140:141], %[h1] offset:2048\n\t"
      "v_mfma_f32_16x16x32_bf16 v[64:67], v[88:91], v[128:131], 0\n\t"
      "ds_read_b64_tr_b16 v[142:143], %[h1] offset:3072\n\t"
      "ds_read_b64_tr_b16 v[104:105], %[a2] offset:4096\n\t"
      "v_mfma_f32_16x16x32_bf16 v[68:71], v[88:91], v[132:135], 0\n\t"
      "buffer_load_dwordx4 %[vl], %[rs], %[tmp] offen offset:1024 sc1 lds\n\t"
      "ds_read_b64_tr_b16 v[106:107], %[a2] offset:6144\n\t"
      "v_mfma_f32_16x16x32_bf16 v[72:75], v[92:95], v[128:131], 0\n\t"
      "ds_read_b64_tr_b16 v[108:109], %[a3] offset:4096\n\t"
      "v_mfma_f32_16x16x32_bf16 v[76:79], v[92:95], v[132:135], 0\n\t"
      "ds_read_b64_tr_b16 v[110:111], %[a3] offset:6144\n\t"
      "s_waitcnt lgkmcnt(6)\n\t"
      "v_mfma_f32_16x16x32_bf16 a[240:243], v[124:127], v[136:139], a[240:243]\n\t"
      "s_waitcnt lgkmcnt(4)\n\t"
      "s_add_u32 m0, %[mR], 2048\n\t"
      "s_add_u32 %[tmp], %[sH], 13312\n\t"
      "buffer_load_dwordx4 %[vl], %[rs], %[tmp] offen offset:0 sc1 lds\n\t"
      "v_mfma_f32_16x16x32_bf16 a[244:247], v[124:127], v[140:143], a[244:247]\n\t"
      "ds_read_b64_tr_b16 v[80:81], %[a0] offset:8192\n\t"
      "ds_read_b64_tr_b16 v[82:83], %[a0] offset:10240\n\t"
      "v_mfma_f32_16x16x32_bf16 v[48:51], v[96:99], v[136:139], v[48:51]\n\t"
      "ds_read_b64_tr_b16 v[84:85], %[a1] offset:8192\n\t"
      "v_mfma_f32_16x16x32_bf16 v[52:55], v[96:99], v[140:143], v[52:55]\n\t"
      "s_waitcnt vmcnt(8)\n\t"
      "ds_read_b64_tr_b16 v[86:87], %[a1] offset:10240\n\t"
      "v_mfma_f32_16x16x32_bf16 v[56:59], v[100:103], v[136:139], v[56:59]\n\t"
      "ds_read_b64_tr_b16 v[144:145], %[h0] offset:4096\n\t"
      "ds_read_b64_tr_b16 v[146:147], %[h0] offset:5120\n\t"
      "v_mfma_f32_16x16x32_bf16 v[60:63], v[100:103], v[140:143], v[60:63]\n\t"
      "ds_read_b64_tr_b16 v[148:149], %[h1] offset:4096\n\t"
      "s_waitcnt lgkmcnt(9)\n\t"
      "v_mfma_f32_16x16x32_bf16 v[64:67], v[104:107], v[136:139], v[64:67]\n\t"
      "ds_read_b64_tr_b16 v[150:151], %[h1] offset:5120\n\t"
      "ds_read_b64_tr_b16 v[88:89], %[a2] offset:8192\n\t"
      "v_mfma_f32_16x16x32_bf16 v[68:71], v[104:107], v[140:143], v[68:71]\n\t"
      "buffer_load_dwordx4 %[vl], %[rs], %[tmp] offen offset:1024 sc1 lds\n\t"
      "ds_read_b64_tr_b16 v[90:91], %[a2] offset:10240\n\t"
      "s_waitcnt lgkmcnt(10)\n\t"
      "v_mfma_f32_16x16x32_bf16 v[72:75], v[108:111], v[136:139], v[72:75]\n\t"
      "ds_read_b64_tr_b16 v[92:93], %[a3] offset:8192\n\t"
      "v_mfma_f32_16x16x32_bf16 v[76:79], v[108:111], v[140:143], v[76:79]\n\t"
      "ds_read_b64_tr_b16 v[94:95], %[a3] offset:10240\n\t"
      "s_waitcnt lgkmcnt(6)\n\t"
      "v_mfma_f32_16x16x32_bf16 a[240:243], v[124:127], v[144:147], a[240:243]\n\t"
      "s_waitcnt lgkmcnt(4)\n\t"
      "s_add_u32 m0, %[mR], 4096\n\t"
      "s_add_u32 %[tmp], %[sH], 15360\n\t"
      "buffer_load_dwordx4 %[vl], %[rs], %[tmp] offen offset:0 sc1 lds\n\t"
      "v_mfma_f32_16x16x32_bf16 a[244:247], v[124:127], v[148:151], a[244:247]\n\t"
      "ds_read_b64_tr_b16 v[96:97], %[a0] offset:12288\n\t"
      "ds_read_b64_tr_b16 v[98:99], %[a0] offset:14336\n\t"
      "v_mfma_f32_16x16x32_bf16 v[48:51], v[80:83], v[144:147], v[48:51]\n\t"
      "ds_read_b64_tr_b16 v[100:101], %[a1] offset:12288\n\t"
      "v_mfma_f32_16x16x32_bf16 v[52:55], v[80:83], v[148:151], v[52:55]\n\t"
      "s_waitcnt vmcnt(8)\n\t"
      "ds_read_b64_tr_b16 v[102:103], %[a1] offset:14336\n\t"
      "v_mfma_f32_16x16x32_bf16 v[56:59], v[84:87], v[144:147], v[56:59]\n\t"
      "ds_read_b64_tr_b16 v[152:153], %[h0] offset:6144\n\t"
      "ds_read_b64_tr_b16 v[154:155], %[h0] offset:7168\n\t"
      "v_mfma_f32_16x16x32_bf16 v[60:63], v[84:87], v[148:151], v[60:63]\n\t"
      "ds_read_b64_tr_b16 v[156:157], %[h1] offset:6144\n\t"
      "s_waitcnt lgkmcnt(9)\n\t"
      "v_mfma_f32_16x16x32_bf16 v[64:67], v[88:91], v[144:147], v[64:67]\n\t"
      "ds_read_b64_tr_b16 v[158:159], %[h1] offset:7168\n\t"
      "ds_read_b64_tr_b16 v[104:105], %[a2] offset:12288\n\t"
      "v_mfma_f32_16x16x32_bf16 v[68:71], v[88:91], v[148:151], v[68:71]\n\t"
      "buffer_load_dwordx4 %[vl], %[rs], %[tmp] offen offset:1024 sc1 lds\n\t"
      "ds_read_b64_tr_b16 v[106:107], %[a2] offset:14336\n\t"
      "s_waitcnt lgkmcnt(10)\n\t"
      "v_mfma_f32_16x16x32_bf16 v[72:75], v[92:95], v[144:147], v[72:75]\n\t"
      "ds_read_b64_tr_b16 v[108:109], %[a3] offset:12288\n\t"
      "v_mfma_f32_16x16x32_bf16 v[76:79], v[92:95], v[148:151], v[76:79]\n\t"
      "ds_read_b64_tr_b16 v[110:111], %[a3] offset:14336\n\t"
      "s_waitcnt lgkmcnt(6)\n\t"
      "v_mfma_f32_16x16x32_bf16 a[240:243], v[124:127], v[152:155], a[240:243]\n\t"
      "s_waitcnt lgkmcnt(4)\n\t"
      "s_add_u32 m0, %[mR], 6144\n\t"
      "s_add_u32 %[tmp], %[sH], 17408\n\t"
      "buffer_load_dwordx4 %[vl], %[rs], %[tmp] offen offset:0 sc1 lds\n\t"
      "v_mfma_f32_16x16x32_bf16 a[244:247], v[124:127], v[156:159], a[244:247]\n\t"
      "ds_read_b64_tr_b16 v[80:81], %[a0] offset:16384\n\t"
      "ds_read_b64_tr_b16 v[82:83], %[a0] offset:18432\n\t"
      "v_mfma_f32_16x16x32_bf16 v[48:51], v[96:99], v[152:155], v[48:51]\n\t"
      "ds_read_b64_tr_b16 v[84:85], %[a1] offset:16384\n\t"
      "v_mfma_f32_16x16x32_bf16 v[52:55], v[96:99], v[156:159], v[52:55]\n\t"
      "s_waitcnt vmcnt(8)\n\t"
      "ds_read_b64_tr_b16 v[86:87], %[a1] offset:18432\n\t"
      "v_mfma_f32_16x16x32_bf16 v[56:59], v[100:103], v[152:155], v[56:59]\n\t"
      "ds_read_b64_tr_b16 v[160:161], %[h0] offset:8192\n\t"
      "ds_read_b64_tr_b16 v[162:163], %[h0] offset:9216\n\t"
      "v_mfma_f32_16x16x32_bf16 v[60:63], v[100:103], v[156:159], v[60:63]\n\t"
      "ds_read_b64_tr_b16 v[164:165], %[h1] offset:8192\n\t"
      "s_waitcnt lgkmcnt(9)\n\t"
      "v_mfma_f32_16x16x32_bf16 v[64:67], v[104:107], v[152:155], v[64:67]\n\t"
      "ds_read_b64_tr_b16 v[166:167], %[h1] offset:9216\n\t"
      "ds_read_b64_tr_b16 v[88:89], %[a2] offset:16384\n\t"
      "v_mfma_f32_16x16x32_bf16 v[68:71], v[104:107], v[156:159], v[68:71]\n\t"
      "buffer_load_dwordx4 %[vl], %[rs], %[tmp] offen offset:1024 sc1 lds\n\t"
      "ds_read_b64_tr_b16 v[90:91], %[a2] offset:18432\n\t"
      "s_waitcnt lgkmcnt(10)\n\t"
      "v_mfma_f32_16x16x32_bf16 v[72:75], v[108:111], v[152:155], v[72:75]\n\t"
      "ds_read_b64_tr_b16 v[92:93], %[a3] offset:16384\n\t"
      "v_mfma_f32_16x16x32_bf16 v[76:79], v[108:111], v[156:159], v[76:79]\n\t"
      "ds_read_b64_tr_b16 v[94:95], %[a3] offset:18432\n\t"
      "s_waitcnt lgkmcnt(6)\n\t"
      "v_mfma_f32_16x16x32_bf16 a[240:243], v[124:127], v[160:163], a[240:243]\n\t"
      "s_waitcnt lgkmcnt(4)\n\t"
      "s_add_u32 m0, %[mR], 8192\n\t"
      "s_add_u32 %[tmp], %[sH], 19456\n\t"
      "buffer_load_dwordx4 %[vl], %[rs], %[tmp] offen offset:0 sc1 lds\n\t"
      "v_mfma_f32_16x16x32_bf16 a[244:247], v[124:127], v[164:167], a[244:247]\n\t"
      "ds_read_b64_tr_b16 v[96:97], %[a0] offset:20480\n\t"
      "ds_read_b64_tr_b16 v[98:99], %[a0] offset:22528\n\t"
      "v_mfma_f32_16x16x32_bf16 v[48:51], v[80:83], v[160:163], v[48:51]\n\t"
      "ds_read_b64_tr_b16 v[100:101], %[a1] offset:20480\n\t"
      "v_mfma_f32_16x16x32_bf16 v[52:55], v[80:83], v[164:167], v[52:55]\n\t"
      "s_waitcnt vmcnt(8)\n\t"
      "ds_read_b64_tr_b16 v[102:103], %[a1] offset:22528\n\t"
      "v_mfma_f32_16x16x32_bf16 v[56:59], v[84:87], v[160:163], v[56:59]\n\t"
      "ds_read_b64_tr_b16 v[168:169], %[h0] offset:10240\n\t"
      "ds_read_b64_tr_b16 v[170:171], %[h0] offset:0\n\t"
      "v_mfma_f32_16x16x32_bf16 v[60:63], v[84:87], v[164:167], v[60:63]\n\t"
      "ds_read_b64_tr_b16 v[172:173], %[h1] offset:10240\n\t"
      "s_waitcnt lgkmcnt(9)\n\t"
      "v_mfma_f32_16x16x32_bf16 v[64:67], v[88:91], v[160:163], v[64:67]\n\t"
      "ds_read_b64_tr_b16 v[174:175], %[h1] offset:0\n\t"
      "ds_read_b64_tr_b16 v[104:105], %[a2] offset:20480\n\t"
      "v_mfma_f32_16x16x32_bf16 v[68:71], v[88:91], v[164:167], v[68:71]\n\t"
      "buffer_load_dwordx4 %[vl], %[rs], %[tmp] offen offset:1024 sc1 lds\n\t"
      "ds_read_b64_tr_b16 v[106:107], %[a2] offset:22528\n\t"
      "s_waitcnt lgkmcnt(10)\n\t"
      "v_mfma_f32_16x16x32_bf16 v[72:75], v[92:95], v[160:163], v[72:75]\n\t"
      "ds_read_b64_tr_b16 v[108:109], %[a3] offset:20480\n\t"
      "v_mfma_f32_16x16x32_bf16 v[76:79], v[92:95], v[164:167], v[76:79]\n\t"
      "ds_read_b64_tr_b16 v[110:111], %[a3] offset:22528\n\t"
      "s_waitcnt lgkmcnt(6)\n\t"
      "v_mfma_f32_16x16x32_bf16 a[240:243], v[124:127], v[168:171], a[240:243]\n\t"
      "s_waitcnt lgkmcnt(4)\n\t"
      "s_add_u32 m0, %[mR], 10240\n\t"
      "s_add_u32 %[tmp], %[sH], 21504\n\t"
      "buffer_load_dwordx4 %[vl], %[rs], %[tmp] offen offset:0 sc1 lds\n\t"
      "v_mfma_f32_16x16x32_bf16 a[244:247], v[124:127], v[172:175], a[244:247]\n\t"
      "ds_read_b64_tr_b16 v[80:81], %[a0] offset:24576\n\t"
      "ds_read_b64_tr_b16 v[82:83], %[a0] offset:26624\n\t"
      "v_mfma_f32_16x16x32_bf16 v[48:51], v[96:99], v[168:171], v[48:51]\n\t"
      "ds_read_b64_tr_b16 v[84:85], %[a1] offset:24576\n\t"
      "v_mfma_f32_16x16x32_bf16 v[52:55], v[96:99], v[172:175], v[52:55]\n\t"
      "s_waitcnt vmcnt(8)\n\t"
      "ds_read_b64_tr_b16 v[86:87], %[a1] offset:26624\n\t"
      "v_mfma_f32_16x16x32_bf16 v[56:59], v[100:103], v[168:171], v[56:59]\n\t"
      "ds_read_b64_tr_b16 v[176:177], %[h0] offset:1024\n\t"
      "ds_read_b64_tr_b16 v[178:179], %[h0] offset:2048\n\t"
      "v_mfma_f32_16x16x32_bf16 v[60:63], v[100:103], v[172:175], v[60:63]\n\t"
      "ds_read_b64_tr_b16 v[180:181], %[h1] offset:1024\n\t"
      "s_waitcnt lgkmcnt(9)\n\t"
      "v_mfma_f32_16x16x32_bf16 v[64:67], v[104:107], v[168:171], v[64:67]\n\t"
      "ds_read_b64_tr_b16 v[182:183], %[h1] offset:2048\n\t"
      "ds_read_b64_tr_b16 v[88:89], %[a2] offset:24576\n\t"
      "v_mfma_f32_16x16x32_bf16 v[68:71], v[104:107], v[172:175], v[68:71]\n\t"
      "s_add_u32 m0, %[mR], 0\n\t"
      "s_add_u32 %[tmp], %[sH], 22528\n\t"
      "buffer_load_dwordx4 %[vl], %[rs], %[tmp] offen offset:0 sc1 lds\n\t"
      "ds_read_b64_tr_b16 v[90:91], %[a2] offset:26624\n\t"
      "s_waitcnt lgkmcnt(10)\n\t"
      "v_mfma_f32_16x16x32_bf16 v[72:75], v[108:111], v[168:171], v[72:75]\n\t"
      "ds_read_b64_tr_b16 v[92:93], %[a3] offset:24576\n\t"
      "v_mfma_f32_16x16x32_bf16 v[76:79], v[108:111], v[172:175], v[76:79]\n\t"
      "ds_read_b64_tr_b16 v[94:95], %[a3] offset:26624\n\t"
      "s_waitcnt lgkmcnt(6)\n\t"
      "v_mfma_f32_16x16x32_bf16 a[240:243], v[124:127], v[176:179], a[240:243]\n\t"
      "s_waitcnt lgkmcnt(4)\n\t"
      "s_add_u32 m0, %[mR], 1024\n\t"
      "s_add_u32 %[tmp], %[sH], 23552\n\t"
      "buffer_load_dwordx4 %[vl], %[rs], %[tmp] offen offset:0 sc1 lds\n\t"
      "v_mfma_f32_16x16x32_bf16 a[244:247], v[124:127], v[180:183], a[244:247]\n\t"
      "ds_read_b64_tr_b16 v[96:97], %[a0] offset:28672\n\t"
      "ds_read_b64_tr_b16 v[98:99], %[a0] offset:30720\n\t"
      "v_mfma_f32_16x16x32_bf16 v[48:51], v[80:83], v[176:179], v[48:51]\n\t"
      "ds_read_b64_tr_b16 v[100:101], %[a1] offset:28672\n\t"
      "v_mfma_f32_16x16x32_bf16 v[52:55], v[80:83], v[180:183], v[52:55]\n\t"
      "s_waitcnt vmcnt(8)\n\t"
      "ds_read_b64_tr_b16 v[102:103], %[a1] offset:30720\n\t"
      "v_mfma_f32_16x16x32_bf16 v[56:59], v[84:87], v[176:179], v[56:59]\n\t"
      "ds_read_b64_tr_b16 v[184:185], %[h0] offset:3072\n\t"
      "ds_read_b64_tr_b16 v[186:187], %[h0] offset:4096\n\t"
      "v_mfma_f32_16x16x32_bf16 v[60:63], v[84:87], v[180:183], v[60:63]\n\t"
      "ds_read_b64_tr_b16 v[188:189], %[h1] offset:3072\n\t"
      "s_waitcnt lgkmcnt(9)\n\t"
      "v_mfma_f32_16x16x32_bf16 v[64:67], v[88:91], v[176:179], v[64:67]\n\t"
      "ds_read_b64_tr_b16 v[190:191], %[h1] offset:4096\n\t"
      "ds_read_b64_tr_b16 v[104:105], %[a2] offset:28672\n\t"
      "v_mfma_f32_16x16x32_bf16 v[68:71], v[88:91], v[180:183], v[68:71]\n\t"
      "buffer_load_dwordx4 %[vl], %[rs], %[tmp] offen offset:1024 sc1 lds\n\t"
      "ds_read_b64_tr_b16 v[106:107], %[a2] offset:30720\n\t"
      "s_waitcnt lgkmcnt(10)\n\t"
      "v_mfma_f32_16x16x32_bf16 v[72:75], v[92:95], v[176:179], v[72:75]\n\t"
      "ds_read_b64_tr_b16 v[108:109], %[a3] offset:28672\n\t"
      "v_mfma_f32_16x16x32_bf16 v[76:79], v[92:95], v[180:183], v[76:79]\n\t"
      "ds_read_b64_tr_b16 v[110:111], %[a3] offset:30720\n\t"
      "s_waitcnt lgkmcnt(6)\n\t"
      "v_mfma_f32_16x16x32_bf16 a[240:243], v[124:127], v[184:187], a[240:243]\n\t"
      "s_waitcnt lgkmcnt(4)\n\t"
      "s_add_u32 m0, %[mR], 3072\n\t"
      "s_add_u32 %[tmp], %[sH], 25600\n\t"
      "buffer_load_dwordx4 %[vl], %[rs], %[tmp] offen offset:0 sc1 lds\n\t"
      "v_mfma_f32_16x16x32_bf16 a[244:247], v[124:127], v[188:191], a[244:247]\n\t"
      "ds_read_b64_tr_b16 v[80:81], %[a0] offset:32768\n\t"
      "ds_read_b64_tr_b16 v[82:83], %[a0] offset:34816\n\t"
      "v_mfma_f32_16x16x32_bf16 v[48:51], v[96:99], v[184:187], v[48:51]\n\t"
      "ds_read_b64_tr_b16 v[84:85], %[a1] offset:32768\n\t"
      "v_mfma_f32_16x16x32_bf16 v[52:55], v[96:99], v[188:191], v[52:55]\n\t"
      "s_waitcnt vmcnt(8)\n\t"
      "ds_read_b64_tr_b16 v[86:87], %[a1] offset:34816\n\t"
      "v_mfma_f32_16x16x32_bf16 v[56:59], v[100:103], v[184:187], v[56:59]\n\t"
      "ds_read_b64_tr_b16 v[192:193], %[h0] offset:5120\n\t"
      "ds_read_b64_tr_b16 v[194:195], %[h0] offset:6144\n\t"
      "v_mfma_f32_16x16x32_bf16 v[60:63], v[100:103], v[188:191], v[60:63]\n\t"
      "ds_read_b64_tr_b16 v[196:197], %[h1] offset:5120\n\t"
      "s_waitcnt lgkmcnt(9)\n\t"
      "v_mfma_f32_16x16x32_bf16 v[64:67], v[104:107], v[184:187], v[64:67]\n\t"
      "ds_read_b64_tr_b16 v[198:199], %[h1] offset:6144\n\t"
      "ds_read_b64_tr_b16 v[88:89], %[a2] offset:32768\n\t"
      "v_mfma_f32_16x16x32_bf16 v[68:71], v[104:107], v[188:191], v[68:71]\n\t"
      "buffer_load_dwordx4 %[vl], %[rs], %[tmp] offen offset:1024 sc1 lds\n\t"
      "ds_read_b64_tr_b16 v[90:91], %[a2] offset:34816\n\t"
      "s_waitcnt lgkmcnt(10)\n\t"
      "v_mfma_f32_16x16x32_bf16 v[72:75], v[108:111], v[184:187], v[72:75]\n\t"
      "ds_read_b64_tr_b16 v[92:93], %[a3] offset:32768\n\t"
      "v_mfma_f32_16x16x32_bf16 v[76:79], v[108:111], v[188:191], v[76:79]\n\t"
      "ds_read_b64_tr_b16 v[94:95], %[a3] offset:34816\n\t"
      "s_waitcnt lgkmcnt(6)\n\t"
      "v_mfma_f32_16x16x32_bf16 a[240:243], v[124:127], v[192:195], a[240:243]\n\t"
      "s_waitcnt lgkmcnt(4)\n\t"
      "s_add_u32 m0, %[mR], 5120\n\t"
      "s_add_u32 %[tmp], %[sH], 27648\n\t"
      "buffer_load_dwordx4 %[vl], %[rs], %[tmp] offen offset:0 sc1 lds\n\t"
      "v_mfma_f32_16x16x32_bf16 a[244:247], v[124:127], v[196:199], a[244:247]\n\t"
      "ds_read_b64_tr_b16 v[96:97], %[a0] offset:36864\n\t"
      "ds_read_b64_tr_b16 v[98:99], %[a0] offset:38912\n\t"
      "v_mfma_f32_16x16x32_bf16 v[48:51], v[80:83], v[192:195], v[48:51]\n\t"
      "ds_read_b64_tr_b16 v[100:101], %[a1] offset:36864\n\t"
      "v_mfma_f32_16x16x32_bf16 v[52:55], v[80:83], v[196:199], v[52:55]\n\t"
      "s_waitcnt vmcnt(8)\n\t"
      "ds_read_b64_tr_b16 v[102:103], %[a1] offset:38912\n\t"
      "v_mfma_f32_16x16x32_bf16 v[56:59], v[84:87], v[192:195], v[56:59]\n\t"
      "ds_read_b64_tr_b16 v[200:201], %[h0] offset:7168\n\t"
      "ds_read_b64_tr_b16 v[202:203], %[h0] offset:8192\n\t"
      "v_mfma_f32_16x16x32_bf16 v[60:63], v[84:87], v[196:199], v[60:63]\n\t"
      "ds_read_b64_tr_b16 v[204:205], %[h1] offset:7168\n\t"
      "s_waitcnt lgkmcnt(9)\n\t"
      "v_mfma_f32_16x16x32_bf16 v[64:67], v[88:91], v[192:195], v[64:67]\n\t"
      "ds_read_b64_tr_b16 v[206:207], %[h1] offset:8192\n\t"
      "ds_read_b64_tr_b16 v[104:105], %[a2] offset:36864\n\t"
      "v_mfma_f32_16x16x32_bf16 v[68:71], v[88:91], v[196:199], v[68:71]\n\t"
      "buffer_load_dwordx4 %[vl], %[rs], %[tmp] offen offset:1024 sc1 lds\n\t"
      "ds_read_b64_tr_b16 v[106:107], %[a2] offset:38912\n\t"
      "s_waitcnt lgkmcnt(10)\n\t"
      "v_mfma_f32_16x16x32_bf16 v[72:75], v[92:95], v[192:195], v[72:75]\n\t"
      "ds_read_b64_tr_b16 v[108:109], %[a3] offset:36864\n\t"
      "v_mfma_f32_16x16x32_bf16 v[76:79], v[92:95], v[196:199], v[76:79]\n\t"
      "ds_read_b64_tr_b16 v[110:111], %[a3] offset:38912\n\t"
      "s_waitcnt lgkmcnt(6)\n\t"
      "v_mfma_f32_16x16x32_bf16 a[240:243], v[124:127], v[200:203], a[240:243]\n\t"
      "s_waitcnt lgkmcnt(4)\n\t"
      "s_add_u32 m0, %[mR], 7168\n\t"
      "s_add_u32 %[tmp], %[sH], 29696\n\t"
      "buffer_load_dwordx4 %[vl], %[rs], %[tmp] offen offset:0 sc1 lds\n\t"
      "v_mfma_f32_16x16x32_bf16 a[244:247], v[124:127], v[204:207], a[244:247]\n\t"
      "ds_read_b64_tr_b16 v[80:81], %[a0] offset:40960\n\t"
      "ds_read_b64_tr_b16 v[82:83], %[a0] offset:43008\n\t"
      "v_mfma_f32_16x16x32_bf16 v[48:51], v[96:99], v[200:203], v[48:51]\n\t"
      "ds_read_b64_tr_b16 v[84:85], %[a1] offset:40960\n\t"
      "v_mfma_f32_16x16x32_bf16 v[52:55], v[96:99], v[204:207], v[52:55]\n\t"
      "s_waitcnt vmcnt(8)\n\t"
      "ds_read_b64_tr_b16 v[86:87], %[a1] offset:43008\n\t"
      "v_mfma_f32_16x16x32_bf16 v[56:59], v[100:103], v[200:203], v[56:59]\n\t"
      "ds_read_b64_tr_b16 v[208:209], %[h0] offset:9216\n\t"
      "ds_read_b64_tr_b16 v[210:211], %[h0] offset:10240\n\t"
      "v_mfma_f32_16x16x32_bf16 v[60:63], v[100:103], v[204:207], v[60:63]\n\t"
      "ds_read_b64_tr_b16 v[212:213], %[h1] offset:9216\n\t"
      "s_waitcnt lgkmcnt(9)\n\t"
      "v_mfma_f32_16x16x32_bf16 v[64:67], v[104:107], v[200:203], v[64:67]\n\t"
      "ds_read_b64_tr_b16 v[214:215], %[h1] offset:10240\n\t"
      "ds_read_b64_tr_b16 v[88:89], %[a2] offset:40960\n\t"
      "v_mfma_f32_16x16x32_bf16 v[68:71], v[104:107], v[204:207], v[68:71]\n\t"
      "buffer_load_dwordx4 %[vl], %[rs], %[tmp] offen offset:1024 sc1 lds\n\t"
      "ds_read_b64_tr_b16 v[90:91], %[a2] offset:43008\n\t"
      "s_waitcnt lgkmcnt(10)\n\t"
      "v_mfma_f32_16x16x32_bf16 v[72:75], v[108:111], v[200:203], v[72:75]\n\t"
      "ds_read_b64_tr_b16 v[92:93], %[a3] offset:40960\n\t"
      "v_mfma_f32_16x16x32_bf16 v[76:79], v[108:111], v[204:207], v[76:79]\n\t"
      "ds_read_b64_tr_b16 v[94:95], %[a3] offset:43008\n\t"
      "s_waitcnt lgkmcnt(6)\n\t"
      "v_mfma_f32_16x16x32_bf16 a[240:243], v[124:127], v[208:211], a[240:243]\n\t"
      "s_waitcnt lgkmcnt(4)\n\t"
      "s_add_u32 m0, %[mR], 9216\n\t"
      "s_add_u32 %[tmp], %[sH], 31744\n\t"
      "buffer_load_dwordx4 %[vl], %[rs], %[tmp] offen offset:0 sc1 lds\n\t"
      "v_mfma_f32_16x16x32_bf16 a[244:247], v[124:127], v[212:215], a[244:247]\n\t"
      "ds_read_b64_tr_b16 v[96:97], %[a0] offset:45056\n\t"
      "ds_read_b64_tr_b16 v[98:99], %[a0] offset:47104\n\t"
      "v_mfma_f32_16x16x32_bf16 v[48:51], v[80:83], v[208:211], v[48:51]\n\t"
      "ds_read_b64_tr_b16 v[100:101], %[a1] offset:45056\n\t"
      "v_mfma_f32_16x16x32_bf16 v[52:55], v[80:83], v[212:215], v[52:55]\n\t"
      "s_waitcnt vmcnt(8)\n\t"
      "ds_read_b64_tr_b16 v[102:103], %[a1] offset:47104\n\t"
      "v_mfma_f32_16x16x32_bf16 v[56:59], v[84:87], v[208:211], v[56:59]\n\t"
      "ds_read_b64_tr_b16 v[216:217], %[h0] offset:0\n\t"
      "ds_read_b64_tr_b16 v[218:219], %[h0] offset:1024\n\t"
      "v_mfma_f32_16x16x32_bf16 v[60:63], v[84:87], v[212:215], v[60:63]\n\t"
      "ds_read_b64_tr_b16 v[220:221], %[h1] offset:0\n\t"
      "s_waitcnt lgkmcnt(9)\n\t"
      "v_mfma_f32_16x16x32_bf16 v[64:67], v[88:91], v[208:211], v[64:67]\n\t"
      "ds_read_b64_tr_b16 v[222:223], %[h1] offset:1024\n\t"
      "ds_read_b64_tr_b16 v[104:105], %[a2] offset:45056\n\t"
      "v_mfma_f32_16x16x32_bf16 v[68:71], v[88:91], v[212:215], v[68:71]\n\t"
      "ds_read_b64_tr_b16 v[106:107], %[a2] offset:47104\n\t"
      "s_waitcnt lgkmcnt(10)\n\t"
      "v_mfma_f32_16x16x32_bf16 v[72:75], v[92:95], v[208:211], v[72:75]\n\t"
      "ds_read_b64_tr_b16 v[108:109], %[a3] offset:45056\n\t"
      "v_mfma_f32_16x16x32_bf16 v[76:79], v[92:95], v[212:215], v[76:79]\n\t"
      "ds_read_b64_tr_b16 v[110:111], %[a3] offset:47104\n\t"
      "s_waitcnt lgkmcnt(6)\n\t"
      "v_mfma_f32_16x16x32_bf16 a[240:243], v[124:127], v[216:219], a[240:243]\n\t"
      "s_waitcnt lgkmcnt(4)\n\t"
      "v_mfma_f32_16x16x32_bf16 a[244:247], v[124:127], v[220:223], a[244:247]\n\t"
      "ds_read_b64_tr_b16 v[80:81], %[a0] offset:49152\n\t"
      "ds_read_b64_tr_b16 v[82:83], %[a0] offset:51200\n\t"
      "v_mfma_f32_16x16x32_bf16 v[48:51], v[96:99], v[216:219], v[48:51]\n\t"
      "ds_read_b64_tr_b16 v[84:85], %[a1] offset:49152\n\t"
      "v_mfma_f32_16x16x32_bf16 v[52:55], v[96:99], v[220:223], v[52:55]\n\t"
      "s_waitcnt vmcnt(6)\n\t"
      "ds_read_b64_tr_b16 v[86:87], %[a1] offset:51200\n\t"
      "v_mfma_f32_16x16x32_bf16 v[56:59], v[100:103], v[216:219], v[56:59]\n\t"
      "ds_read_b64_tr_b16 v[224:225], %[h0] offset:2048\n\t"
      "ds_read_b64_tr_b16 v[226:227], %[h0] offset:3072\n\t"
      "v_mfma_f32_16x16x32_bf16 v[60:63], v[100:103], v[220:223], v[60:63]\n\t"
      "ds_read_b64_tr_b16 v[228:229], %[h1] offset:2048\n\t"
      "s_waitcnt lgkmcnt(9)\n\t"
      "v_mfma_f32_16x16x32_bf16 v[64:67], v[104:107], v[216:219], v[64:67]\n\t"
      "ds_read_b64_tr_b16 v[230:231], %[h1] offset:3072\n\t"
      "ds_read_b64_tr_b16 v[88:89], %[a2] offset:49152\n\t"
      "v_mfma_f32_16x16x32_bf16 v[68:71], v[104:107], v[220:223], v[68:71]\n\t"
      "ds_read_b64_tr_b16 v[90:91], %[a2] offset:51200\n\t"
      "s_waitcnt lgkmcnt(10)\n\t"
      "v_mfma_f32_16x16x32_bf16 v[72:75], v[108:111], v[216:219], v[72:75]\n\t"
      "ds_read_b64_tr_b16 v[92:93], %[a3] offset:49152\n\t"
      "v_mfma_f32_16x16x32_bf16 v[76:79], v[108:111], v[220:223], v[76:79]\n\t"
      "ds_read_b64_tr_b16 v[94:95], %[a3] offset:51200\n\t"
      "s_waitcnt lgkmcnt(6)\n\t"
      "v_mfma_f32_16x16x32_bf16 a[240:243], v[124:127], v[224:227], a[240:243]\n\t"
      "s_waitcnt lgkmcnt(4)\n\t"
      "v_mfma_f32_16x16x32_bf16 a[244:247], v[124:127], v[228:231], a[244:247]\n\t"
      "ds_read_b64_tr_b16 v[96:97], %[a0] offset:53248\n\t"
      "ds_read_b64_tr_b16 v[98:99], %[a0] offset:55296\n\t"
      "v_mfma_f32_16x16x32_bf16 v[48:51], v[80:83], v[224:227], v[48:51]\n\t"
      "ds_read_b64_tr_b16 v[100:101], %[a1] offset:53248\n\t"
      "v_mfma_f32_16x16x32_bf16 v[52:55], v[80:83], v[228:231], v[52:55]\n\t"
      "s_waitcnt vmcnt(4)\n\t"
      "ds_read_b64_tr_b16 v[102:103], %[a1] offset:55296\n\t"
      "v_mfma_f32_16x16x32_bf16 v[56:59], v[84:87], v[224:227], v[56:59]\n\t"
      "ds_read_b64_tr_b16 v[232:233], %[h0] offset:4096\n\t"
      "ds_read_b64_tr_b16 v[234:235], %[h0] offset:5120\n\t"
      "v_mfma_f32_16x16x32_bf16 v[60:63], v[84:87], v[228:231], v[60:63]\n\t"
      "ds_read_b64_tr_b16 v[236:237], %[h1] offset:4096\n\t"
      "s_waitcnt lgkmcnt(9)\n\t"
      "v_mfma_f32_16x16x32_bf16 v[64:67], v[88:91], v[224:227], v[64:67]\n\t"
      "ds_read_b64_tr_b16 v[238:239], %[h1] offset:5120\n\t"
      "ds_read_b64_tr_b16 v[104:105], %[a2] offset:53248\n\t"
      "v_mfma_f32_16x16x32_bf16 v[68:71], v[88:91], v[228:231], v[68:71]\n\t"
      "ds_read_b64_tr_b16 v[106:107], %[a2] offset:55296\n\t"
      "s_waitcnt lgkmcnt(10)\n\t"
      "v_mfma_f32_16x16x32_bf16 v[72:75], v[92:95], v[224:227], v[72:75]\n\t"
      "ds_read_b64_tr_b16 v[108:109], %[a3] offset:53248\n\t"
      "v_mfma_f32_16x16x32_bf16 v[76:79], v[92:95], v[228:231], v[76:79]\n\t"
      "ds_read_b64_tr_b16 v[110:111], %[a3] offset:55296\n\t"
      "s_waitcnt lgkmcnt(6)\n\t"
      "v_mfma_f32_16x16x32_bf16 a[240:243], v[124:127], v[232:235], a[240:243]\n\t"
      "s_waitcnt lgkmcnt(4)\n\t"
      "v_mfma_f32_16x16x32_bf16 a[244:247], v[124:127], v[236:239], a[244:247]\n\t"
      "ds_read_b64_tr_b16 v[80:81], %[a0] offset:57344\n\t"
      "ds_read_b64_tr_b16 v[82:83], %[a0] offset:59392\n\t"
      "v_mfma_f32_16x16x32_bf16 v[48:51], v[96:99], v[232:235], v[48:51]\n\t"
      "ds_read_b64_tr_b16 v[84:85], %[a1] offset:57344\n\t"
      "v_mfma_f32_16x16x32_bf16 v[52:55], v[96:99], v[236:239], v[52:55]\n\t"
      "s_waitcnt vmcnt(2)\n\t"
      "ds_read_b64_tr_b16 v[86:87], %[a1] offset:59392\n\t"
      "v_mfma_f32_16x16x32_bf16 v[56:59], v[100:103], v[232:235], v[56:59]\n\t"
      "ds_read_b64_tr_b16 v[240:241], %[h0] offset:6144\n\t"
      "ds_read_b64_tr_b16 v[242:243], %[h0] offset:7168\n\t"
      "v_mfma_f32_16x16x32_bf16 v[60:63], v[100:103], v[236:239], v[60:63]\n\t"
      "ds_read_b64_tr_b16 v[244:245], %[h1] offset:6144\n\t"
      "s_waitcnt lgkmcnt(9)\n\t"
      "v_mfma_f32_16x16x32_bf16 v[64:67], v[104:107], v[232:235], v[64:67]\n\t"
      "ds_read_b64_tr_b16 v[246:247], %[h1] offset:7168\n\t"
      "ds_read_b64_tr_b16 v[88:89], %[a2] offset:57344\n\t"
      "v_mfma_f32_16x16x32_bf16 v[68:71], v[104:107], v[236:239], v[68:71]\n\t"
      "ds_read_b64_tr_b16 v[90:91], %[a2] offset:59392\n\t"
      "s_waitcnt lgkmcnt(10)\n\t"
      "v_mfma_f32_16x16x32_bf16 v[72:75], v[108:111], v[232:235], v[72:75]\n\t"
      "ds_read_b64_tr_b16 v[92:93], %[a3] offset:57344\n\t"
      "v_mfma_f32_16x16x32_bf16 v[76:79], v[108:111], v[236:239], v[76:79]\n\t"
      "ds_read_b64_tr_b16 v[94:95], %[a3] offset:59392\n\t"
      "s_waitcnt lgkmcnt(6)\n\t"
      "v_mfma_f32_16x16x32_bf16 a[240:243], v[124:127], v[240:243], a[240:243]\n\t"
      "s_waitcnt lgkmcnt(4)\n\t"
      "v_mfma_f32_16x16x32_bf16 a[244:247], v[124:127], v[244:247], a[244:247]\n\t"
      "ds_read_b64_tr_b16 v[96:97], %[a0] offset:61440\n\t"
      "ds_read_b64_tr_b16 v[98:99], %[a0] offset:63488\n\t"
      "v_mfma_f32_16x16x32_bf16 v[48:51], v[80:83], v[240:243], v[48:51]\n\t"
      "ds_read_b64_tr_b16 v[100:101], %[a1] offset:61440\n\t"
      "v_mfma_f32_16x16x32_bf16 v[52:55], v[80:83], v[244:247], v[52:55]\n\t"
      "s_waitcnt vmcnt(0)\n\t"
      "ds_read_b64_tr_b16 v[102:103], %[a1] offset:63488\n\t"
      "v_mfma_f32_16x16x32_bf16 v[56:59], v[84:87], v[240:243], v[56:59]\n\t"
      "ds_read_b64_tr_b16 v[248:249], %[h0] offset:8192\n\t"
      "ds_read_b64_tr_b16 v[250:251], %[h0] offset:9216\n\t"
      "s_add_u32 %[tw], %[sV], 8192\n\t"
      "buffer_load_dwordx4 a[176:179], %[w0], %[rws], %[tw] offen offset:0 sc1\n\t"
      "v_mfma_f32_16x16x32_bf16 v[60:63], v[84:87], v[244:247], v[60:63]\n\t"
      "ds_read_b64_tr_b16 v[252:253], %[h1] offset:8192\n\t"
      "s_waitcnt lgkmcnt(9)\n\t"
      "v_mfma_f32_16x16x32_bf16 v[64:67], v[88:91], v[240:243], v[64:67]\n\t"
      "ds_read_b64_tr_b16 v[254:255], %[h1] offset:9216\n\t"
      "ds_read_b64_tr_b16 v[104:105], %[a2] offset:61440\n\t"
      "v_mfma_f32_16x16x32_bf16 v[68:71], v[88:91], v[244:247], v[68:71]\n\t"
      "ds_read_b64_tr_b16 v[106:107], %[a2] offset:63488\n\t"
      "buffer_load_dwordx4 a[180:183], %[w1], %[rws], %[tw] offen offset:0 sc1\n\t"
      "s_waitcnt lgkmcnt(10)\n\t"
      "v_mfma_f32_16x16x32_bf16 v[72:75], v[92:95], v[240:243], v[72:75]\n\t"
      "ds_read_b64_tr_b16 v[108:109], %[a3] offset:61440\n\t"
      "ds_read_b64_tr_b16 v[112:113], %[ab] offset:0\n\t"
      "ds_read_b64_tr_b16 v[114:115], %[ab] offset:1536\n\t"
      "ds_read_b64_tr_b16 v[116:117], %[ab] offset:32\n\t"
      "v_mfma_f32_16x16x32_bf16 v[76:79], v[92:95], v[244:247], v[76:79]\n\t"
      "ds_read_b64_tr_b16 v[110:111], %[a3] offset:63488\n\t"
      "ds_read_b64_tr_b16 v[118:119], %[ab] offset:1568\n\t"
      "ds_read_b64_tr_b16 v[120:121], %[ab] offset:64\n\t"
      "ds_read_b64_tr_b16 v[122:123], %[ab] offset:1600\n\t"
      "s_waitcnt lgkmcnt(12)\n\t"
      "v_mfma_f32_16x16x32_bf16 a[240:243], v[124:127], v[248:251], a[240:243]\n\t"
      "buffer_load_dwordx4 a[184:187], %[w0], %[rws], %[tw] offen offset:2048 sc1\n\t"
      "s_waitcnt lgkmcnt(10)\n\t"
      "v_mfma_f32_16x16x32_bf16 a[244:247], v[124:127], v[252:255], a[244:247]\n\t"
      "v_mfma_f32_16x16x32_bf16 v[48:51], v[96:99], v[248:251], v[48:51]\n\t"
      "v_mfma_f32_16x16x32_bf16 v[52:55], v[96:99], v[252:255], v[52:55]\n\t"
      "buffer_load_dwordx4 a[188:191], %[w1], %[rws], %[tw] offen offset:2048 sc1\n\t"
      "v_mfma_f32_16x16x32_bf16 v[56:59], v[100:103], v[248:251], v[56:59]\n\t"
      "ds_read_b128 v[80:83], %[cs] offset:0\n\t"
      "v_mfma_f32_16x16x32_bf16 v[60:63], v[100:103], v[252:255], v[60:63]\n\t"
      "ds_read_b128 v[84:87], %[cs] offset:64\n\t"
      "s_waitcnt lgkmcnt(10)\n\t"
      "v_mfma_f32_16x16x32_bf16 v[64:67], v[104:107], v[248:251], v[64:67]\n\t"
      "ds_read_b128 v[88:91], %[cs] offset:128\n\t"
      "v_mfma_f32_16x16x32_bf16 v[68:71], v[104:107], v[252:255], v[68:71]\n\t"
      "ds_read_b128 v[92:95], %[cs] offset:192\n\t"
      "s_waitcnt lgkmcnt(7)\n\t"
      "v_mfma_f32_16x16x32_bf16 v[72:75], v[108:111], v[248:251], v[72:75]\n\t"
      "v_mfma_f32_16x16x32_bf16 v[76:79], v[108:111], v[252:255], v[76:79]\n\t"
      "v_accvgpr_read_b32 v44, a240\n\t"
      "v_accvgpr_read_b32 v46, a244\n\t"
      "v_mul_f32 v44, 0x3b000000, v44\n\t"
      "v_mul_f32 v46, 0x3b000000, v46\n\t"
      "s_mov_b32 vcc_lo, 0xffff0000\n\t"
      "s_mov_b32 vcc_hi, 0xffff0000\n\t"
      "v_cndmask_b32 v45, v44, v46, vcc\n\t"
      "v_cvt_pk_bf16_f32 v44, -v44, -v46\n\t"
      "v_and_b32 v46, 0xffff0000, v44\n\t"
      "v_lshlrev_b32 v44, 16, v44\n\t"
      "s_waitcnt lgkmcnt(0)\n\t"
      "v_pk_fma_f32 v[48:49], v[44:45], v[80:81], v[48:49] op_sel_hi:[0,1,1]\n\t"
      "v_pk_fma_f32 v[50:51], v[44:45], v[82:83], v[50:51] op_sel_hi:[0,1,1]\n\t"
      "v_pk_fma_f32 v[52:53], v[46:47], v[80:81], v[52:53] op_sel_hi:[0,1,1]\n\t"
      "v_pk_fma_f32 v[54:55], v[46:47], v[82:83], v[54:55] op_sel_hi:[0,1,1]\n\t"
      "v_cvt_pk_bf16_f32 v48, v48, v49\n\t"
      "v_cvt_pk_bf16_f32 v49, v50, v51\n\t"
      "v_cvt_pk_bf16_f32 v50, v52, v53\n\t"
      "v_cvt_pk_bf16_f32 v51, v54, v55\n\t"
      "v_pk_max_i16 v48, v48, 0\n\t"
      "v_pk_max_i16 v50, v50, 0\n\t"
      "v_pk_max_i16 v49, v49, 0\n\t"
      "v_pk_max_i16 v51, v51, 0\n\t"
      "v_permlane16_swap_b32 v48, v50\n\t"
      "s_nop 0\n\t"
      "v_permlane16_swap_b32 v49, v51\n\t"
      "buffer_store_dwordx4 v[48:51], %[hf], %[rs], 0 offen offset:0 sc1\n\t"
      "v_pk_fma_f32 v[56:57], v[44:45], v[84:85], v[56:57] op_sel_hi:[0,1,1]\n\t"
      "v_pk_fma_f32 v[58:59], v[44:45], v[86:87], v[58:59] op_sel_hi:[0,1,1]\n\t"
      "v_pk_fma_f32 v[60:61], v[46:47], v[84:85], v[60:61] op_sel_hi:[0,1,1]\n\t"
      "v_pk_fma_f32 v[62:63], v[46:47], v[86:87], v[62:63] op_sel_hi:[0,1,1]\n\t"
      "v_cvt_pk_bf16_f32 v56, v56, v57\n\t"
      "v_cvt_pk_bf16_f32 v57, v58, v59\n\t"
      "v_cvt_pk_bf16_f32 v58, v60, v61\n\t"
      "v_cvt_pk_bf16_f32 v59, v62, v63\n\t"
      "v_pk_max_i16 v56, v56, 0\n\t"
      "v_pk_max_i16 v58, v58, 0\n\t"
      "v_pk_max_i16 v57, v57, 0\n\t"
      "v_pk_max_i16 v59, v59, 0\n\t"
      "v_permlane16_swap_b32 v56, v58\n\t"
      "s_nop 0\n\t"
      "v_permlane16_swap_b32 v57, v59\n\t"
      "buffer_store_dwordx4 v[56:59], %[hf], %[rs], 0 offen offset:1024 sc1\n\t"
      "v_pk_fma_f32 v[64:65], v[44:45], v[88:89], v[64:65] op_sel_hi:[0,1,1]\n\t"
      "v_pk_fma_f32 v[66:67], v[44:45], v[90:91], v[66:67] op_sel_hi:[0,1,1]\n\t"
      "v_pk_fma_f32 v[68:69], v[46:47], v[88:89], v[68:69] op_sel_hi:[0,1,1]\n\t"
      "v_pk_fma_f32 v[70:71], v[46:47], v[90:91], v[70:71] op_sel_hi:[0,1,1]\n\t"
      "v_cvt_pk_bf16_f32 v64, v64, v65\n\t"
      "v_cvt_pk_bf16_f32 v65, v66, v67\n\t"
      "v_cvt_pk_bf16_f32 v66, v68, v69\n\t"
      "v_cvt_pk_bf16_f32 v67, v70, v71\n\t"
      "v_pk_max_i16 v64, v64, 0\n\t"
      "v_pk_max_i16 v66, v66, 0\n\t"
      "v_pk_max_i16 v65, v65, 0\n\t"
      "v_pk_max_i16 v67, v67, 0\n\t"
      "v_permlane16_swap_b32 v64, v66\n\t"
      "s_nop 0\n\t"
      "v_permlane16_swap_b32 v65, v67\n\t"
      "buffer_store_dwordx4 v[64:67], %[hf], %[rs], 0 offen offset:2048 sc1\n\t"
      "v_pk_fma_f32 v[72:73], v[44:45], v[92:93], v[72:73] op_sel_hi:[0,1,1]\n\t"
      "v_pk_fma_f32 v[74:75], v[44:45], v[94:95], v[74:75] op_sel_hi:[0,1,1]\n\t"
      "v_pk_fma_f32 v[76:77], v[46:47], v[92:93], v[76:77] op_sel_hi:[0,1,1]\n\t"
      "v_pk_fma_f32 v[78:79], v[46:47], v[94:95], v[78:79] op_sel_hi:[0,1,1]\n\t"
      "v_cvt_pk_bf16_f32 v72, v72, v73\n\t"
      "v_cvt_pk_bf16_f32 v73, v74, v75\n\t"
      "v_cvt_pk_bf16_f32 v74, v76, v77\n\t"
      "v_cvt_pk_bf16_f32 v75, v78, v79\n\t"
      "v_pk_max_i16 v72, v72, 0\n\t"
      "v_pk_max_i16 v74, v74, 0\n\t"
      "v_pk_max_i16 v73, v73, 0\n\t"
      "v_pk_max_i16 v75, v75, 0\n\t"
      "v_permlane16_swap_b32 v72, v74\n\t"
      "s_nop 0\n\t"
      "v_permlane16_swap_b32 v73, v75\n\t"
      "buffer_store_dwordx4 v[72:75], %[hf], %[rs], 0 offen offset:3072 sc1\n\t"
      "ds_read_b64_tr_b16 v[80:81], %[ab] offset:3072\n\t"
      "ds_read_b64_tr_b16 v[82:83], %[ab] offset:4608\n\t"
      "ds_read_b64_tr_b16 v[84:85], %[ab] offset:3104\n\t"
      "ds_read_b64_tr_b16 v[86:87], %[ab] offset:4640\n\t"
      "ds_read_b64_tr_b16 v[88:89], %[ab] offset:3136\n\t"
      "ds_read_b64_tr_b16 v[90:91], %[ab] offset:4672\n\t"
        : [tmp] "=&s"(tmp), [tw] "=&s"(tw)
        : [a0] "v"(aA), [a1] "v"(aA ^ 32u), [a2] "v"(aA ^ 64u), [a3] "v"(aA ^ 96u), [ab] "v"(aB), [h0] "v"(hq), [h1] "v"(hq ^ 32u), [mR] "s"(mR), [sH] "s"(sH),
          [vl] "v"(lane * 16), [rs] "s"(rws), [rws] "s"(rws), [sW] "s"(sW), [sV] "s"(sV), [w0] "v"(vW), [w1] "v"(vW ^ 1024u), [cp] "v"(L_RING + 11264 + (lane & 31) * 16),
          [cq] "v"(ring + 11712 + (lane & 31) * 16), [cs] "v"(csp), [hf] "v"(hf + 4 * mem * 1024)
        : "memory", "scc", "vcc");
  }
  asm volatile("s_barrier" ::: "memory");
  const unsigned vf = lane == 0 ? WS_FC + (grp * 32 + mem) * 128 + w * 8 : 0x7ffffff0u;
  const unsigned vp = lane < 8 ? WS_FC + (grp * 32 + 4 * w + (lane >> 1)) * 128 + (2 * th + (lane & 1)) * 8 : 0x7ffffff0u;
  const v2u non = {nlo, nhi};
  const unsigned hb = WS_HF + (grp * 4 + 2 * th) * (112 * 1024) + lane * 16;
  {
    unsigned tmp, tw;
    asm volatile(
      "s_waitcnt lgkmcnt(6)\n\t"
      "v_mfma_f32_16x16x32_bf16 v[48:51], v[112:115], v[128:131], 0\n\t"
      "ds_read_b64_tr_b16 v[92:93], %[ab] offset:6144\n\t"
      "v_mfma_f32_16x16x32_bf16 v[52:55], v[112:115], v[132:135], 0\n\t"
      "ds_read_b64_tr_b16 v[94:95], %[ab] offset:7680\n\t"
      "v_mfma_f32_16x16x32_bf16 v[56:59], v[116:119], v[128:131], 0\n\t"
      "ds_read_b64_tr_b16 v[96:97], %[ab] offset:6176\n\t"
      "s_add_u32 m0, %[mR], 8192\n\t"
      "s_nop 0\n\t"
      "buffer_load_dwordx4 %[vl], %[rs], %[sR] offen sc1 lds\n\t"
      "v_mfma_f32_16x16x32_bf16 v[60:63], v[116:119], v[132:135], 0\n\t"
      "ds_read_b64_tr_b16 v[98:99], %[ab] offset:7712\n\t"
      "v_mfma_f32_16x16x32_bf16 v[64:67], v[120:123], v[128:131], 0\n\t"
      "ds_read_b64_tr_b16 v[100:101], %[ab] offset:6208\n\t"
      "v_mfma_f32_16x16x32_bf16 v[68:71], v[120:123], v[132:135], 0\n\t"
      "ds_read_b64_tr_b16 v[102:103], %[ab] offset:7744\n\t"
      "s_waitcnt lgkmcnt(6)\n\t"
      "v_mfma_f32_16x16x32_bf16 v[48:51], v[80:83], v[136:139], v[48:51]\n\t"
      "ds_read_b64_tr_b16 v[112:113], %[ab] offset:9216\n\t"
      "v_mfma_f32_16x16x32_bf16 v[52:55], v[80:83], v[140:143], v[52:55]\n\t"
      "ds_read_b64_tr_b16 v[114:115], %[ab] offset:10752\n\t"
      "v_mfma_f32_16x16x32_bf16 v[56:59], v[84:87], v[136:139], v[56:59]\n\t"
      "ds_read_b64_tr_b16 v[116:117], %[ab] offset:9248\n\t"
      "buffer_load_dwordx4 %[vl], %[rs], %[sR] offen offset:1024 sc1 lds\n\t"
      "v_mfma_f32_16x16x32_bf16 v[60:63], v[84:87], v[140:143], v[60:63]\n\t"
      "ds_read_b64_tr_b16 v[118:119], %[ab] offset:10784\n\t"
      "v_mfma_f32_16x16x32_bf16 v[64:67], v[88:91], v[136:139], v[64:67]\n\t"
      "ds_read_b64_tr_b16 v[120:121], %[ab] offset:9280\n\t"
      "v_mfma_f32_16x16x32_bf16 v[68:71], v[88:91], v[140:143], v[68:71]\n\t"
      "ds_read_b64_tr_b16 v[122:123], %[ab] offset:10816\n\t"
      "s_waitcnt lgkmcnt(6)\n\t"
      "v_mfma_f32_16x16x32_bf16 v[48:51], v[92:95], v[144:147], v[48:51]\n\t"
      "ds_read_b64_tr_b16 v[80:81], %[ab] offset:12288\n\t"
      "v_mfma_f32_16x16x32_bf16 v[52:55], v[92:95], v[148:151], v[52:55]\n\t"
      "ds_read_b64_tr_b16 v[82:83], %[ab] offset:13824\n\t"
      "v_mfma_f32_16x16x32_bf16 v[56:59], v[96:99], v[144:147], v[56:59]\n\t"
      "ds_read_b64_tr_b16 v[84:85], %[ab] offset:12320\n\t"
      "v_mfma_f32_16x16x32_bf16 v[60:63], v[96:99], v[148:151], v[60:63]\n\t"
      "ds_read_b64_tr_b16 v[86:87], %[ab] offset:13856\n\t"
      "v_mfma_f32_16x16x32_bf16 v[64:67], v[100:103], v[144:147], v[64:67]\n\t"
      "ds_read_b64_tr_b16 v[88:89], %[ab] offset:12352\n\t"
      "v_mfma_f32_16x16x32_bf16 v[68:71], v[100:103], v[148:151], v[68:71]\n\t"
      "ds_read_b64_tr_b16 v[90:91], %[ab] offset:13888\n\t"
      "s_waitcnt lgkmcnt(6)\n\t"
      "v_mfma_f32_16x16x32_bf16 v[48:51], v[112:115], v[152:155], v[48:51]\n\t"
      "ds_read_b64_tr_b16 v[92:93], %[ab] offset:15360\n\t"
      "v_mfma_f32_16x16x32_bf16 v[52:55], v[112:115], v[156:159], v[52:55]\n\t"
      "ds_read_b64_tr_b16 v[94:95], %[ab] offset:16896\n\t"
      "v_mfma_f32_16x16x32_bf16 v[56:59], v[116:119], v[152:155], v[56:59]\n\t"
      "ds_read_b64_tr_b16 v[96:97], %[ab] offset:15392\n\t"
      "v_mfma_f32_16x16x32_bf16 v[60:63], v[116:119], v[156:159], v[60:63]\n\t"
      "ds_read_b64_tr_b16 v[98:99], %[ab] offset:16928\n\t"
      "v_mfma_f32_16x16x32_bf16 v[64:67], v[120:123], v[152:155], v[64:67]\n\t"
      "ds_read_b64_tr_b16 v[100:101], %[ab] offset:15424\n\t"
      "v_mfma_f32_16x16x32_bf16 v[68:71], v[120:123], v[156:159], v[68:71]\n\t"
      "ds_read_b64_tr_b16 v[102:103], %[ab] offset:16960\n\t"
      "s_waitcnt lgkmcnt(6)\n\t"
      "v_mfma_f32_16x16x32_bf16 v[48:51], v[80:83], v[160:163], v[48:51]\n\t"
      "ds_read_b64_tr_b16 v[112:113], %[ab] offset:18432\n\t"
      "v_mfma_f32_16x16x32_bf16 v[52:55], v[80:83], v[164:167], v[52:55]\n\t"
      "ds_read_b64_tr_b16 v[114:115], %[ab] offset:19968\n\t"
      "v_mfma_f32_16x16x32_bf16 v[56:59], v[84:87], v[160:163], v[56:59]\n\t"
      "ds_read_b64_tr_b16 v[116:117], %[ab] offset:18464\n\t"
      "v_mfma_f32_16x16x32_bf16 v[60:63], v[84:87], v[164:167], v[60:63]\n\t"
      "ds_read_b64_tr_b16 v[118:119], %[ab] offset:20000\n\t"
      "v_mfma_f32_16x16x32_bf16 v[64:67], v[88:91], v[160:163], v[64:67]\n\t"
      "ds_read_b64_tr_b16 v[120:121], %[ab] offset:18496\n\t"
      "v_mfma_f32_16x16x32_bf16 v[68:71], v[88:91], v[164:167], v[68:71]\n\t"
      "ds_read_b64_tr_b16 v[122:123], %[ab] offset:20032\n\t"
      "s_waitcnt lgkmcnt(6)\n\t"
      "v_mfma_f32_16x16x32_bf16 v[48:51], v[92:95], v[168:171], v[48:51]\n\t"
      "ds_read_b64_tr_b16 v[80:81], %[ab] offset:21504\n\t"
      "v_mfma_f32_16x16x32_bf16 v[52:55], v[92:95], v[172:175], v[52:55]\n\t"
      "ds_read_b64_tr_b16 v[82:83], %[ab] offset:23040\n\t"
      "v_mfma_f32_16x16x32_bf16 v[56:59], v[96:99], v[168:171], v[56:59]\n\t"
      "ds_read_b64_tr_b16 v[84:85], %[ab] offset:21536\n\t"
      "v_mfma_f32_16x16x32_bf16 v[60:63], v[96:99], v[172:175], v[60:63]\n\t"
      "ds_read_b64_tr_b16 v[86:87], %[ab] offset:23072\n\t"
      "v_mfma_f32_16x16x32_bf16 v[64:67], v[100:103], v[168:171], v[64:67]\n\t"
      "ds_read_b64_tr_b16 v[88:89], %[ab] offset:21568\n\t"
      "v_mfma_f32_16x16x32_bf16 v[68:71], v[100:103], v[172:175], v[68:71]\n\t"
      "ds_read_b64_tr_b16 v[90:91], %[ab] offset:23104\n\t"
      "s_waitcnt lgkmcnt(6)\n\t"
      "v_mfma_f32_16x16x32_bf16 v[48:51], v[112:115], v[176:179], v[48:51]\n\t"
      "ds_read_b64_tr_b16 v[92:93], %[ab] offset:24576\n\t"
      "v_mfma_f32_16x16x32_bf16 v[52:55], v[112:115], v[180:183], v[52:55]\n\t"
      "ds_read_b64_tr_b16 v[94:95], %[ab] offset:26112\n\t"
      "v_mfma_f32_16x16x32_bf16 v[56:59], v[116:119], v[176:179], v[56:59]\n\t"
      "ds_read_b64_tr_b16 v[96:97], %[ab] offset:24608\n\t"
      "v_mfma_f32_16x16x32_bf16 v[60:63], v[116:119], v[180:183], v[60:63]\n\t"
      "ds_read_b64_tr_b16 v[98:99], %[ab] offset:26144\n\t"
      "v_mfma_f32_16x16x32_bf16 v[64:67], v[120:123], v[176:179], v[64:67]\n\t"
      "ds_read_b64_tr_b16 v[100:101], %[ab] offset:24640\n\t"
      "v_mfma_f32_16x16x32_bf16 v[68:71], v[120:123], v[180:183], v[68:71]\n\t"
      "ds_read_b64_tr_b16 v[102:103], %[ab] offset:26176\n\t"
      "s_waitcnt lgkmcnt(6)\n\t"
      "v_mfma_f32_16x16x32_bf16 v[48:51], v[80:83], v[184:187], v[48:51]\n\t"
      "ds_read_b64_tr_b16 v[112:113], %[ab] offset:27648\n\t"
      "v_mfma_f32_16x16x32_bf16 v[52:55], v[80:83], v[188:191], v[52:55]\n\t"
      "ds_read_b64_tr_b16 v[114:115], %[ab] offset:29184\n\t"
      "v_mfma_f32_16x16x32_bf16 v[56:59], v[84:87], v[184:187], v[56:59]\n\t"
      "ds_read_b64_tr_b16 v[116:117], %[ab] offset:27680\n\t"
      "v_mfma_f32_16x16x32_bf16 v[60:63], v[84:87], v[188:191], v[60:63]\n\t"
      "ds_read_b64_tr_b16 v[118:119], %[ab] offset:29216\n\t"
      "v_mfma_f32_16x16x32_bf16 v[64:67], v[88:91], v[184:187], v[64:67]\n\t"
      "ds_read_b64_tr_b16 v[120:121], %[ab] offset:27712\n\t"
      "v_mfma_f32_16x16x32_bf16 v[68:71], v[88:91], v[188:191], v[68:71]\n\t"
      "ds_read_b64_tr_b16 v[122:123], %[ab] offset:29248\n\t"
      "s_waitcnt lgkmcnt(6)\n\t"
      "s_waitcnt vmcnt(0)\n\t"
      "buffer_store_dwordx2 %[non], %[vf], %[rs], 0 offen sc1\n\t"
      "v_mfma_f32_16x16x32_bf16 v[48:51], v[92:95], v[192:195], v[48:51]\n\t"
      "ds_read_b64_tr_b16 v[80:81], %[ab] offset:30720\n\t"
      "s_add_u32 %[tw], %[sV], 12288\n\t"
      "buffer_load_dwordx4 a[192:195], %[w0], %[rws], %[tw] offen offset:0 sc1\n\t"
      "v_mfma_f32_16x16x32_bf16 v[52:55], v[92:95], v[196:199], v[52:55]\n\t"
      "ds_read_b64_tr_b16 v[82:83], %[ab] offset:32256\n\t"
      "v_mfma_f32_16x16x32_bf16 v[56:59], v[96:99], v[192:195], v[56:59]\n\t"
      "ds_read_b64_tr_b16 v[84:85], %[ab] offset:30752\n\t"
      "v_mfma_f32_16x16x32_bf16 v[60:63], v[96:99], v[196:199], v[60:63]\n\t"
      "ds_read_b64_tr_b16 v[86:87], %[ab] offset:32288\n\t"
      "buffer_load_dwordx4 a[196:199], %[w1], %[rws], %[tw] offen offset:0 sc1\n\t"
      "v_mfma_f32_16x16x32_bf16 v[64:67], v[100:103], v[192:195], v[64:67]\n\t"
      "ds_read_b64_tr_b16 v[88:89], %[ab] offset:30784\n\t"
      "v_mfma_f32_16x16x32_bf16 v[68:71], v[100:103], v[196:199], v[68:71]\n\t"
      "ds_read_b64_tr_b16 v[90:91], %[ab] offset:32320\n\t"
      "ds_read_b64_tr_b16 v[72:73], %[ra] offset:8192\n\t"
      "ds_read_b64_tr_b16 v[74:75], %[ra] offset:8704\n\t"
      "ds_read_b64_tr_b16 v[76:77], %[ra] offset:9216\n\t"
      "ds_read_b64_tr_b16 v[78:79], %[ra] offset:9728\n\t"
      "s_waitcnt lgkmcnt(10)\n\t"
      "v_mfma_f32_16x16x32_bf16 v[48:51], v[112:115], v[200:203], v[48:51]\n\t"
      "ds_read_b64_tr_b16 v[92:93], %[ab] offset:33792\n\t"
      "v_mfma_f32_16x16x32_bf16 v[52:55], v[112:115], v[204:207], v[52:55]\n\t"
      "ds_read_b64_tr_b16 v[94:95], %[ab] offset:35328\n\t"
      "v_mfma_f32_16x16x32_bf16 v[56:59], v[116:119], v[200:203], v[56:59]\n\t"
      "ds_read_b64_tr_b16 v[96:97], %[ab] offset:33824\n\t"
      "buffer_load_dwordx4 a[200:203], %[w0], %[rws], %[tw] offen offset:2048 sc1\n\t"
      "v_mfma_f32_16x16x32_bf16 v[60:63], v[116:119], v[204:207], v[60:63]\n\t"
      "ds_read_b64_tr_b16 v[98:99], %[ab] offset:35360\n\t"
      "v_mfma_f32_16x16x32_bf16 v[64:67], v[120:123], v[200:203], v[64:67]\n\t"
      "ds_read_b64_tr_b16 v[100:101], %[ab] offset:33856\n\t"
      "v_mfma_f32_16x16x32_bf16 v[68:71], v[120:123], v[204:207], v[68:71]\n\t"
      "ds_read_b64_tr_b16 v[102:103], %[ab] offset:35392\n\t"
      "s_waitcnt lgkmcnt(6)\n\t"
      "v_mfma_f32_16x16x32_bf16 v[48:51], v[80:83], v[208:211], v[48:51]\n\t"
      "ds_read_b64_tr_b16 v[112:113], %[ab] offset:36864\n\t"
      "buffer_load_dwordx4 a[204:207], %[w1], %[rws], %[tw] offen offset:2048 sc1\n\t"
      "v_mfma_f32_16x16x32_bf16 v[52:55], v[80:83], v[212:215], v[52:55]\n\t"
      "ds_read_b64_tr_b16 v[114:115], %[ab] offset:38400\n\t"
      "v_mfma_f32_16x16x32_bf16 v[56:59], v[84:87], v[208:211], v[56:59]\n\t"
      "ds_read_b64_tr_b16 v[116:117], %[ab] offset:36896\n\t"
      "v_mfma_f32_16x16x32_bf16 v[60:63], v[84:87], v[212:215], v[60:63]\n\t"
      "ds_read_b64_tr_b16 v[118:119], %[ab] offset:38432\n\t"
      "s_add_u32 %[tw], %[sV], 16384\n\t"
      "buffer_load_dwordx4 a[208:211], %[w0], %[rws], %[tw] offen offset:0 sc1\n\t"
      "v_mfma_f32_16x16x32_bf16 v[64:67], v[88:91], v[208:211], v[64:67]\n\t"
      "ds_read_b64_tr_b16 v[120:121], %[ab] offset:36928\n\t"
      "v_mfma_f32_16x16x32_bf16 v[68:71], v[88:91], v[212:215], v[68:71]\n\t"
      "ds_read_b64_tr_b16 v[122:123], %[ab] offset:38464\n\t"
      "s_waitcnt lgkmcnt(6)\n\t"
      "v_mfma_f32_16x16x32_bf16 v[48:51], v[92:95], v[216:219], v[48:51]\n\t"
      "ds_read_b64_tr_b16 v[80:81], %[ab] offset:39936\n\t"
      "v_mfma_f32_16x16x32_bf16 v[52:55], v[92:95], v[220:223], v[52:55]\n\t"
      "ds_read_b64_tr_b16 v[82:83], %[ab] offset:41472\n\t"
      "v_mfma_f32_16x16x32_bf16 v[56:59], v[96:99], v[216:219], v[56:59]\n\t"
      "ds_read_b64_tr_b16 v[84:85], %[ab] offset:39968\n\t"
      "buffer_load_dwordx4 a[212:215], %[w1], %[rws], %[tw] offen offset:0 sc1\n\t"
      "v_mfma_f32_16x16x32_bf16 v[60:63], v[96:99], v[220:223], v[60:63]\n\t"
      "ds_read_b64_tr_b16 v[86:87], %[ab] offset:41504\n\t"
      "v_mfma_f32_16x16x32_bf16 v[64:67], v[100:103], v[216:219], v[64:67]\n\t"
      "ds_read_b64_tr_b16 v[88:89], %[ab] offset:40000\n\t"
      "v_mfma_f32_16x16x32_bf16 v[68:71], v[100:103], v[220:223], v[68:71]\n\t"
      "ds_read_b64_tr_b16 v[90:91], %[ab] offset:41536\n\t"
      "s_waitcnt lgkmcnt(6)\n\t"
      "v_mfma_f32_16x16x32_bf16 v[48:51], v[112:115], v[224:227], v[48:51]\n\t"
      "ds_read_b64_tr_b16 v[92:93], %[ab] offset:43008\n\t"
      "buffer_load_dwordx4 a[216:219], %[w0], %[rws], %[tw] offen offset:2048 sc1\n\t"
      "v_mfma_f32_16x16x32_bf16 v[52:55], v[112:115], v[228:231], v[52:55]\n\t"
      "ds_read_b64_tr_b16 v[94:95], %[ab] offset:44544\n\t"
      "v_mfma_f32_16x16x32_bf16 v[56:59], v[116:119], v[224:227], v[56:59]\n\t"
      "ds_read_b64_tr_b16 v[96:97], %[ab] offset:43040\n\t"
      "v_mfma_f32_16x16x32_bf16 v[60:63], v[116:119], v[228:231], v[60:63]\n\t"
      "ds_read_b64_tr_b16 v[98:99], %[ab] offset:44576\n\t"
      "buffer_load_dwordx4 a[220:223], %[w1], %[rws], %[tw] offen offset:2048 sc1\n\t"
      "v_mfma_f32_16x16x32_bf16 v[64:67], v[120:123], v[224:227], v[64:67]\n\t"
      "ds_read_b64_tr_b16 v[100:101], %[ab] offset:43072\n\t"
      "v_mfma_f32_16x16x32_bf16 v[68:71], v[120:123], v[228:231], v[68:71]\n\t"
      "ds_read_b64_tr_b16 v[102:103], %[ab] offset:44608\n\t"
      "s_waitcnt lgkmcnt(6)\n\t"
      "v_mfma_f32_16x16x32_bf16 v[48:51], v[80:83], v[232:235], v[48:51]\n\t"
      "ds_read_b64_tr_b16 v[112:113], %[ab] offset:46080\n\t"
      "v_mfma_f32_16x16x32_bf16 v[52:55], v[80:83], v[236:239], v[52:55]\n\t"
      "ds_read_b64_tr_b16 v[114:115], %[ab] offset:47616\n\t"
      "v_mfma_f32_16x16x32_bf16 v[56:59], v[84:87], v[232:235], v[56:59]\n\t"
      "ds_read_b64_tr_b16 v[116:117], %[ab] offset:46112\n\t"
      "s_add_u32 %[tw], %[sV], 20480\n\t"
      "buffer_load_dwordx4 a[224:227], %[w0], %[rws], %[tw] offen offset:0 sc1\n\t"
      "v_mfma_f32_16x16x32_bf16 v[60:63], v[84:87], v[236:239], v[60:63]\n\t"
      "ds_read_b64_tr_b16 v[118:119], %[ab] offset:47648\n\t"
      "v_mfma_f32_16x16x32_bf16 v[64:67], v[88:91], v[232:235], v[64:67]\n\t"
      "ds_read_b64_tr_b16 v[120:121], %[ab] offset:46144\n\t"
      "v_mfma_f32_16x16x32_bf16 v[68:71], v[88:91], v[236:239], v[68:71]\n\t"
      "ds_read_b64_tr_b16 v[122:123], %[ab] offset:47680\n\t"
      "buffer_load_dwordx2 v[104:105], %[vp], %[rs], 0 offen sc1\n\t"
      "s_waitcnt lgkmcnt(6)\n\t"
      "v_mfma_f32_16x16x32_bf16 v[48:51], v[92:95], v[240:243], v[48:51]\n\t"
      "ds_read_b128 v[80:83], %[cs] offset:256\n\t"
      "buffer_load_dwordx4 a[228:231], %[w1], %[rws], %[tw] offen offset:0 sc1\n\t"
      "v_mfma_f32_16x16x32_bf16 v[52:55], v[92:95], v[244:247], v[52:55]\n\t"
      "ds_read_b128 v[84:87], %[cs] offset:320\n\t"
      "v_mfma_f32_16x16x32_bf16 v[56:59], v[96:99], v[240:243], v[56:59]\n\t"
      "ds_read_b128 v[88:91], %[cs] offset:384\n\t"
      "v_mfma_f32_16x16x32_bf16 v[60:63], v[96:99], v[244:247], v[60:63]\n\t"
      "buffer_load_dwordx4 a[232:235], %[w0], %[rws], %[tw] offen offset:2048 sc1\n\t"
      "v_mfma_f32_16x16x32_bf16 v[64:67], v[100:103], v[240:243], v[64:67]\n\t"
      "v_mfma_f32_16x16x32_bf16 v[68:71], v[100:103], v[244:247], v[68:71]\n\t"
      "s_waitcnt lgkmcnt(3)\n\t"
      "v_mfma_f32_16x16x32_bf16 v[48:51], v[112:115], v[248:251], v[48:51]\n\t"
      "v_mfma_f32_16x16x32_bf16 v[52:55], v[112:115], v[252:255], v[52:55]\n\t"
      "v_mfma_f32_16x16x32_bf16 v[56:59], v[116:119], v[248:251], v[56:59]\n\t"
      "buffer_load_dwordx4 a[236:239], %[w1], %[rws], %[tw] offen offset:2048 sc1\n\t"
      "v_mfma_f32_16x16x32_bf16 v[60:63], v[116:119], v[252:255], v[60:63]\n\t"
      "v_mfma_f32_16x16x32_bf16 v[64:67], v[120:123], v[248:251], v[64:67]\n\t"
      "v_mfma_f32_16x16x32_bf16 v[68:71], v[120:123], v[252:255], v[68:71]\n\t"
      "s_waitcnt lgkmcnt(0)\n\t"
      "s_nop 2\n\t"
      "v_pk_fma_f32 v[48:49], v[44:45], v[80:81], v[48:49] op_sel_hi:[0,1,1]\n\t"
      "v_pk_fma_f32 v[50:51], v[44:45], v[82:83], v[50:51] op_sel_hi:[0,1,1]\n\t"
      "v_pk_fma_f32 v[52:53], v[46:47], v[80:81], v[52:53] op_sel_hi:[0,1,1]\n\t"
      "v_pk_fma_f32 v[54:55], v[46:47], v[82:83], v[54:55] op_sel_hi:[0,1,1]\n\t"
      "v_cvt_pk_bf16_f32 v48, v48, v49\n\t"
      "v_cvt_pk_bf16_f32 v49, v50, v51\n\t"
      "v_cvt_pk_bf16_f32 v50, v52, v53\n\t"
      "v_cvt_pk_bf16_f32 v51, v54, v55\n\t"
      "v_pk_max_i16 v48, v48, 0\n\t"
      "v_pk_max_i16 v50, v50, 0\n\t"
      "v_pk_max_i16 v49, v49, 0\n\t"
      "v_pk_max_i16 v51, v51, 0\n\t"
      "v_permlane16_swap_b32 v48, v50\n\t"
      "s_nop 0\n\t"
      "v_permlane16_swap_b32 v49, v51\n\t"
      "buffer_store_dwordx4 v[48:51], %[hf], %[rs], 0 offen offset:0 sc1\n\t"
      "v_pk_fma_f32 v[56:57], v[44:45], v[84:85], v[56:57] op_sel_hi:[0,1,1]\n\t"
      "v_pk_fma_f32 v[58:59], v[44:45], v[86:87], v[58:59] op_sel_hi:[0,1,1]\n\t"
      "v_pk_fma_f32 v[60:61], v[46:47], v[84:85], v[60:61] op_sel_hi:[0,1,1]\n\t"
      "v_pk_fma_f32 v[62:63], v[46:47], v[86:87], v[62:63] op_sel_hi:[0,1,1]\n\t"
      "v_cvt_pk_bf16_f32 v56, v56, v57\n\t"
      "v_cvt_pk_bf16_f32 v57, v58, v59\n\t"
      "v_cvt_pk_bf16_f32 v58, v60, v61\n\t"
      "v_cvt_pk_bf16_f32 v59, v62, v63\n\t"
      "v_pk_max_i16 v56, v56, 0\n\t"
      "v_pk_max_i16 v58, v58, 0\n\t"
      "v_pk_max_i16 v57, v57, 0\n\t"
      "v_pk_max_i16 v59, v59, 0\n\t"
      "v_permlane16_swap_b32 v56, v58\n\t"
      "s_nop 0\n\t"
      "v_permlane16_swap_b32 v57, v59\n\t"
      "buffer_store_dwordx4 v[56:59], %[hf], %[rs], 0 offen offset:1024 sc1\n\t"
      "v_pk_fma_f32 v[64:65], v[44:45], v[88:89], v[64:65] op_sel_hi:[0,1,1]\n\t"
      "v_pk_fma_f32 v[66:67], v[44:45], v[90:91], v[66:67] op_sel_hi:[0,1,1]\n\t"
      "v_pk_fma_f32 v[68:69], v[46:47], v[88:89], v[68:69] op_sel_hi:[0,1,1]\n\t"
      "v_pk_fma_f32 v[70:71], v[46:47], v[90:91], v[70:71] op_sel_hi:[0,1,1]\n\t"
      "v_cvt_pk_bf16_f32 v64, v64, v65\n\t"
      "v_cvt_pk_bf16_f32 v65, v66, v67\n\t"
      "v_cvt_pk_bf16_f32 v66, v68, v69\n\t"
      "v_cvt_pk_bf16_f32 v67, v70, v71\n\t"
      "v_pk_max_i16 v64, v64, 0\n\t"
      "v_pk_max_i16 v66, v66, 0\n\t"
      "v_pk_max_i16 v65, v65, 0\n\t"
      "v_pk_max_i16 v67, v67, 0\n\t"
      "v_permlane16_swap_b32 v64, v66\n\t"
      "s_nop 0\n\t"
      "v_permlane16_swap_b32 v65, v67\n\t"
      "buffer_store_dwordx4 v[64:67], %[hf], %[rs], 0 offen offset:2048 sc1\n\t"
      "s_barrier\n\t"
        : [tmp] "=&s"(tmp), [tw] "=&s"(tw)
        : [ab] "v"(aB), [mR] "s"(mR), [rs] "s"(rws), [rws] "s"(rws), [sW] "s"(sW), [sV] "s"(sV), [w0] "v"(vW), [w1] "v"(vW ^ 1024u),
          [vl] "v"(lane * 16), [sR] "s"(WS_HT + (4 * grp + 2 * th + sh) * 32768 + (64 * ds + 32 * cw) * 64), [cs] "v"(csp),
          [ra] "v"(ring + (4 * (lane >> 5) + q4) * 64 + (((g4 & 1) ^ (lane >> 5)) * 32) + 8 * p4),
          [hf] "v"(hf + (64 + 3 * mem) * 1024), [vf] "v"(vf), [vp] "v"(vp), [non] "v"(non)
        : "memory", "scc");
  }

  {
    unsigned tmp = 0, cnt = 0;
    const __amdgpu_buffer_rsrc_t ro = __builtin_amdgcn_make_buffer_rsrc((GAS void*)out, 0, 2048 * 512 * 2, 0x00020000);
    asm volatile(
      "s_mov_b32 %[cnt], 0\n\t"
      "v_mov_b32 v92, 0\n\t"
      "v_mov_b32 v93, 0\n\t"
      "v_dot2c_f32_bf16 v92, v128, v128\n\t"
      "v_dot2c_f32_bf16 v93, v132, v132\n\t"
      "v_dot2c_f32_bf16 v92, v129, v129\n\t"
      "v_dot2c_f32_bf16 v93, v133, v133\n\t"
      "v_dot2c_f32_bf16 v92, v130, v130\n\t"
      "v_dot2c_f32_bf16 v93, v134, v134\n\t"
      "v_dot2c_f32_bf16 v92, v131, v131\n\t"
      "v_dot2c_f32_bf16 v93, v135, v135\n\t"
      "s_waitcnt vmcnt(6)\n\t"
      "v_cmp_ne_u64 vcc, v[104:105], %[non]\n\t"
      "s_nop 3\n\t"
      "s_and_b32 vcc_lo, vcc_lo, 0xff\n\t"
      "s_cbranch_scc0 GAOK%=\n\t"
      "GASPIN%=:\n\t"
      "buffer_load_dwordx2 v[104:105], %[vp], %[rs], 0 offen offset:0 sc1\n\t"
      "s_add_u32 %[cnt], %[cnt], 1\n\t"
      "s_waitcnt vmcnt(0)\n\t"
      "buffer_store_dwordx2 %[non], %[vf], %[rs], 0 offen offset:2048 sc1\n\t"
      "v_cmp_ne_u64 vcc, v[104:105], %[non]\n\t"
      "s_nop 3\n\t"
      "s_and_b32 vcc_lo, vcc_lo, 0xff\n\t"
      "s_cbranch_scc0 GAOK%=\n\t"
      "s_cmp_lt_u32 %[cnt], 0x8000\n\t"
      "s_cbranch_scc1 GASPIN%=\n\t"
      "GAOK%=:\n\t"
      "s_add_u32 %[tmp], %[sA], 0\n\t"
      "buffer_load_dwordx4 v[128:131], %[v0], %[rs], %[tmp] offen offset:0 sc1\n\t"
      "buffer_load_dwordx4 v[132:135], %[v1], %[rs], %[tmp] offen offset:0 sc1\n\t"
      "v_dot2c_f32_bf16 v92, v136, v136\n\t"
      "v_dot2c_f32_bf16 v93, v140, v140\n\t"
      "v_dot2c_f32_bf16 v92, v137, v137\n\t"
      "v_dot2c_f32_bf16 v93, v141, v141\n\t"
      "v_dot2c_f32_bf16 v92, v138, v138\n\t"
      "v_dot2c_f32_bf16 v93, v142, v142\n\t"
      "v_dot2c_f32_bf16 v92, v139, v139\n\t"
      "v_dot2c_f32_bf16 v93, v143, v143\n\t"
      "buffer_load_dwordx4 v[136:139], %[v0], %[rs], %[tmp] offen offset:1024 sc1\n\t"
      "v_dot2c_f32_bf16 v92, v168, v168\n\t"
      "v_dot2c_f32_bf16 v93, v172, v172\n\t"
      "v_dot2c_f32_bf16 v92, v169, v169\n\t"
      "v_dot2c_f32_bf16 v93, v173, v173\n\t"
      "v_dot2c_f32_bf16 v92, v170, v170\n\t"
      "v_dot2c_f32_bf16 v93, v174, v174\n\t"
      "v_dot2c_f32_bf16 v92, v171, v171\n\t"
      "v_dot2c_f32_bf16 v93, v175, v175\n\t"
      "v_dot2c_f32_bf16 v92, v176, v176\n\t"
      "v_dot2c_f32_bf16 v93, v180, v180\n\t"
      "v_dot2c_f32_bf16 v92, v177, v177\n\t"
      "buffer_load_dwordx4 v[140:143], %[v1], %[rs], %[tmp] offen offset:1024 sc1\n\t"
      "v_dot2c_f32_bf16 v92, v144, v144\n\t"
      "v_dot2c_f32_bf16 v93, v148, v148\n\t"
      "v_dot2c_f32_bf16 v92, v145, v145\n\t"
      "v_dot2c_f32_bf16 v93, v149, v149\n\t"
      "v_dot2c_f32_bf16 v92, v146, v146\n\t"
      "v_dot2c_f32_bf16 v93, v150, v150\n\t"
      "v_dot2c_f32_bf16 v92, v147, v147\n\t"
      "v_dot2c_f32_bf16 v93, v151, v151\n\t"
      "v_dot2c_f32_bf16 v93, v181, v181\n\t"
      "v_dot2c_f32_bf16 v92, v178, v178\n\t"
      "v_dot2c_f32_bf16 v93, v182, v182\n\t"
      "v_dot2c_f32_bf16 v92, v179, v179\n\t"
      "v_dot2c_f32_bf16 v93, v183, v183\n\t"
      "v_dot2c_f32_bf16 v92, v184, v184\n\t"
      "v_dot2c_f32_bf16 v93, v188, v188\n\t"
      "v_dot2c_f32_bf16 v92, v185, v185\n\t"
      "v_dot2c_f32_bf16 v93, v189, v189\n\t"
      "v_dot2c_f32_bf16 v92, v186, v186\n\t"
      "v_dot2c_f32_bf16 v93, v190, v190\n\t"
      "buffer_load_dwordx4 v[144:147], %[v0], %[rs], %[tmp] offen offset:2048 sc1\n\t"
      "v_dot2c_f32_bf16 v92, v187, v187\n\t"
      "v_dot2c_f32_bf16 v93, v191, v191\n\t"
      "v_dot2c_f32_bf16 v92, v192, v192\n\t"
      "v_dot2c_f32_bf16 v93, v196, v196\n\t"
      "v_dot2c_f32_bf16 v92, v193, v193\n\t"
      "v_dot2c_f32_bf16 v93, v197, v197\n\t"
      "v_dot2c_f32_bf16 v92, v194, v194\n\t"
      "v_dot2c_f32_bf16 v93, v198, v198\n\t"
      "v_dot2c_f32_bf16 v92, v195, v195\n\t"
      "v_dot2c_f32_bf16 v93, v199, v199\n\t"
      "v_dot2c_f32_bf16 v92, v200, v200\n\t"
      "buffer_load_dwordx4 v[148:151], %[v1], %[rs], %[tmp] offen offset:2048 sc1\n\t"
      "v_dot2c_f32_bf16 v92, v152, v152\n\t"
      "v_dot2c_f32_bf16 v93, v156, v156\n\t"
      "v_dot2c_f32_bf16 v92, v153, v153\n\t"
      "v_dot2c_f32_bf16 v93, v157, v157\n\t"
      "v_dot2c_f32_bf16 v92, v154, v154\n\t"
      "v_dot2c_f32_bf16 v93, v158, v158\n\t"
      "v_dot2c_f32_bf16 v92, v155, v155\n\t"
      "v_dot2c_f32_bf16 v93, v159, v159\n\t"
      "v_dot2c_f32_bf16 v93, v204, v204\n\t"
      "v_dot2c_f32_bf16 v92, v201, v201\n\t"
      "v_dot2c_f32_bf16 v93, v205, v205\n\t"
      "v_dot2c_f32_bf16 v92, v202, v202\n\t"
      "v_dot2c_f32_bf16 v93, v206, v206\n\t"
      "v_dot2c_f32_bf16 v92, v203, v203\n\t"
      "v_dot2c_f32_bf16 v93, v207, v207\n\t"
      "v_dot2c_f32_bf16 v92, v208, v208\n\t"
      "v_dot2c_f32_bf16 v93, v212, v212\n\t"
      "v_dot2c_f32_bf16 v92, v209, v209\n\t"
      "v_dot2c_f32_bf16 v93, v213, v213\n\t"
      "buffer_load_dwordx4 v[152:155], %[v0], %[rs], %[tmp] offen offset:3072 sc1\n\t"
      "v_dot2c_f32_bf16 v92, v210, v210\n\t"
      "v_dot2c_f32_bf16 v93, v214, v214\n\t"
      "v_dot2c_f32_bf16 v92, v211, v211\n\t"
      "v_dot2c_f32_bf16 v93, v215, v215\n\t"
      "v_dot2c_f32_bf16 v92, v216, v216\n\t"
      "v_dot2c_f32_bf16 v93, v220, v220\n\t"
      "v_dot2c_f32_bf16 v92, v217, v217\n\t"
      "v_dot2c_f32_bf16 v93, v221, v221\n\t"
      "v_dot2c_f32_bf16 v92, v218, v218\n\t"
      "v_dot2c_f32_bf16 v93, v222, v222\n\t"
      "v_dot2c_f32_bf16 v92, v219, v219\n\t"
      "buffer_load_dwordx4 v[156:159], %[v1], %[rs], %[tmp] offen offset:3072 sc1\n\t"
      "v_dot2c_f32_bf16 v92, v160, v160\n\t"
      "v_dot2c_f32_bf16 v93, v164, v164\n\t"
      "v_dot2c_f32_bf16 v92, v161, v161\n\t"
      "v_dot2c_f32_bf16 v93, v165, v165\n\t"
      "v_dot2c_f32_bf16 v92, v162, v162\n\t"
      "v_dot2c_f32_bf16 v93, v166, v166\n\t"
      "v_dot2c_f32_bf16 v92, v163, v163\n\t"
      "v_dot2c_f32_bf16 v93, v167, v167\n\t"
      "v_dot2c_f32_bf16 v93, v223, v223\n\t"
      "v_dot2c_f32_bf16 v92, v224, v224\n\t"
      "v_dot2c_f32_bf16 v93, v228, v228\n\t"
      "v_dot2c_f32_bf16 v92, v225, v225\n\t"
      "v_dot2c_f32_bf16 v93, v229, v229\n\t"
      "v_dot2c_f32_bf16 v92, v226, v226\n\t"
      "v_dot2c_f32_bf16 v93, v230, v230\n\t"
      "v_dot2c_f32_bf16 v92, v227, v227\n\t"
      "v_dot2c_f32_bf16 v93, v231, v231\n\t"
      "v_dot2c_f32_bf16 v92, v232, v232\n\t"
      "v_dot2c_f32_bf16 v93, v236, v236\n\t"
      "s_add_u32 %[tmp], %[sA], 4096\n\t"
      "buffer_load_dwordx4 v[160:163], %[v0], %[rs], %[tmp] offen offset:0 sc1\n\t"
      "v_dot2c_f32_bf16 v92, v233, v233\n\t"
      "v_dot2c_f32_bf16 v93, v237, v237\n\t"
      "v_dot2c_f32_bf16 v92, v234, v234\n\t"
      "v_dot2c_f32_bf16 v93, v238, v238\n\t"
      "v_dot2c_f32_bf16 v92, v235, v235\n\t"
      "v_dot2c_f32_bf16 v93, v239, v239\n\t"
      "v_dot2c_f32_bf16 v92, v240, v240\n\t"
      "v_dot2c_f32_bf16 v93, v244, v244\n\t"
      "v_dot2c_f32_bf16 v92, v241, v241\n\t"
      "v_dot2c_f32_bf16 v93, v245, v245\n\t"
      "v_dot2c_f32_bf16 v92, v242, v242\n\t"
      "buffer_load_dwordx4 v[164:167], %[v1], %[rs], %[tmp] offen offset:0 sc1\n\t"
      "v_dot2c_f32_bf16 v93, v246, v246\n\t"
      "v_dot2c_f32_bf16 v92, v243, v243\n\t"
      "v_dot2c_f32_bf16 v93, v247, v247\n\t"
      "v_dot2c_f32_bf16 v92, v248, v248\n\t"
      "v_dot2c_f32_bf16 v93, v252, v252\n\t"
      "v_dot2c_f32_bf16 v92, v249, v249\n\t"
      "v_dot2c_f32_bf16 v93, v253, v253\n\t"
      "v_dot2c_f32_bf16 v92, v250, v250\n\t"
      "v_dot2c_f32_bf16 v93, v254, v254\n\t"
      "v_dot2c_f32_bf16 v92, v251, v251\n\t"
      "v_dot2c_f32_bf16 v93, v255, v255\n\t"
      "s_nop 2\n\t"
      "v_permlane16_swap_b32 v92, v93\n\t"
      "v_add_f32 v92, v92, v93\n\t"
      "v_mov_b32 v93, v92\n\t"
      "s_nop 1\n\t"
      "v_permlane32_swap_b32 v92, v93\n\t"
      "v_add_f32 v92, v92, v93\n\t"
      "v_mul_f32 v92, 0x3b000000, v92\n\t"
      "v_fma_f32 v92, -v45, v45, v92\n\t"
      "v_max_f32 v92, 0, v92\n\t"
      "v_cvt_pk_bf16_f32 v92, v92, v92\n\t"
      "v_lshlrev_b32 v92, 16, v92\n\t"
      "v_add_f32 v92, 0x3727c5ac, v92\n\t"
      "v_cvt_pk_bf16_f32 v92, v92, v92\n\t"
      "v_lshlrev_b32 v92, 16, v92\n\t"
      "v_sqrt_f32 v92, v92\n\t"
      "s_nop 0\n\t"
      "v_cvt_pk_bf16_f32 v92, v92, v92\n\t"
      "v_lshlrev_b32 v92, 16, v92\n\t"
      "v_rcp_f32 v92, v92\n\t"
      "s_nop 0\n\t"
      "ds_write_b32 %[prs], v92\n\t"
      "s_waitcnt vmcnt(8)\n\t"
      "buffer_store_dwordx2 %[non], %[vf], %[rs], 0 offen offset:2048 sc1\n\t"
      "v_mfma_f32_32x32x16_bf16 v[80:95], a[16:19], v[132:135], 0\n\t"
      "buffer_load_dwordx4 v[168:171], %[v0], %[rs], %[tmp] offen offset:1024 sc1\n\t"
      "v_mfma_f32_32x32x16_bf16 v[96:111], a[20:23], v[128:131], 0\n\t"
      "v_mfma_f32_32x32x16_bf16 a[240:255], a[20:23], v[132:135], 0\n\t"
      "buffer_load_dwordx4 v[172:175], %[v1], %[rs], %[tmp] offen offset:1024 sc1\n\t"
      "v_mfma_f32_32x32x16_bf16 v[112:127], a[16:19], v[128:131], 0\n\t"
      "s_waitcnt vmcnt(9)\n\t"
      "v_mfma_f32_32x32x16_bf16 v[80:95], a[24:27], v[140:143], v[80:95]\n\t"
      "buffer_load_dwordx4 v[176:179], %[v0], %[rs], %[tmp] offen offset:2048 sc1\n\t"
      "v_mfma_f32_32x32x16_bf16 v[96:111], a[28:31], v[136:139], v[96:111]\n\t"
      "v_mfma_f32_32x32x16_bf16 a[240:255], a[28:31], v[140:143], a[240:255]\n\t"
      "buffer_load_dwordx4 v[180:183], %[v1], %[rs], %[tmp] offen offset:2048 sc1\n\t"
      "v_mfma_f32_32x32x16_bf16 v[112:127], a[24:27], v[136:139], v[112:127]\n\t"
      "s_waitcnt vmcnt(9)\n\t"
      "v_mfma_f32_32x32x16_bf16 v[80:95], a[32:35], v[148:151], v[80:95]\n\t"
      "buffer_load_dwordx4 v[184:187], %[v0], %[rs], %[tmp] offen offset:3072 sc1\n\t"
      "v_mfma_f32_32x32x16_bf16 v[96:111], a[36:39], v[144:147], v[96:111]\n\t"
      "v_mfma_f32_32x32x16_bf16 a[240:255], a[36:39], v[148:151], a[240:255]\n\t"
      "buffer_load_dwordx4 v[188:191], %[v1], %[rs], %[tmp] offen offset:3072 sc1\n\t"
      "v_mfma_f32_32x32x16_bf16 v[112:127], a[32:35], v[144:147], v[112:127]\n\t"
      "s_waitcnt vmcnt(9)\n\t"
      "v_mfma_f32_32x32x16_bf16 v[80:95], a[40:43], v[156:159], v[80:95]\n\t"
      "s_add_u32 %[tmp], %[sA], 8192\n\t"
      "buffer_load_dwordx4 v[192:195], %[v0], %[rs], %[tmp] offen offset:0 sc1\n\t"
      "v_mfma_f32_32x32x16_bf16 v[96:111], a[44:47], v[152:155], v[96:111]\n\t"
      "v_mfma_f32_32x32x16_bf16 a[240:255], a[44:47], v[156:159], a[240:255]\n\t"
      "buffer_load_dwordx4 v[196:199], %[v1], %[rs], %[tmp] offen offset:0 sc1\n\t"
      "v_mfma_f32_32x32x16_bf16 v[112:127], a[40:43], v[152:155], v[112:127]\n\t"
      "s_waitcnt vmcnt(9)\n\t"
      "v_mfma_f32_32x32x16_bf16 v[80:95], a[48:51], v[164:167], v[80:95]\n\t"
      "buffer_load_dwordx4 v[200:203], %[v0], %[rs], %[tmp] offen offset:1024 sc1\n\t"
      "v_mfma_f32_32x32x16_bf16 v[96:111], a[52:55], v[160:163], v[96:111]\n\t"
      "v_mfma_f32_32x32x16_bf16 a[240:255], a[52:55], v[164:167], a[240:255]\n\t"
      "buffer_load_dwordx4 v[204:207], %[v1], %[rs], %[tmp] offen offset:1024 sc1\n\t"
      "v_mfma_f32_32x32x16_bf16 v[112:127], a[48:51], v[160:163], v[112:127]\n\t"
      "s_waitcnt vmcnt(8)\n\t"
      "v_mfma_f32_32x32x16_bf16 v[80:95], a[56:59], v[172:175], v[80:95]\n\t"
      "buffer_load_dwordx4 v[208:211], %[v0], %[rs], %[tmp] offen offset:2048 sc1\n\t"
      "v_mfma_f32_32x32x16_bf16 v[96:111], a[60:63], v[168:171], v[96:111]\n\t"
      "v_mfma_f32_32x32x16_bf16 a[240:255], a[60:63], v[172:175], a[240:255]\n\t"
      "buffer_load_dwordx4 v[212:215], %[v1], %[rs], %[tmp] offen offset:2048 sc1\n\t"
      "v_mfma_f32_32x32x16_bf16 v[112:127], a[56:59], v[168:171], v[112:127]\n\t"
      "s_waitcnt vmcnt(8)\n\t"
      "v_mfma_f32_32x32x16_bf16 v[80:95], a[64:67], v[180:183], v[80:95]\n\t"
      "buffer_load_dwordx4 v[216:219], %[v0], %[rs], %[tmp] offen offset:3072 sc1\n\t"
      "v_mfma_f32_32x32x16_bf16 v[96:111], a[68:71], v[176:179], v[96:111]\n\t"
      "v_mfma_f32_32x32x16_bf16 a[240:255], a[68:71], v[180:183], a[240:255]\n\t"
      "buffer_load_dwordx4 v[220:223], %[v1], %[rs], %[tmp] offen offset:3072 sc1\n\t"
      "v_mfma_f32_32x32x16_bf16 v[112:127], a[64:67], v[176:179], v[112:127]\n\t"
      "buffer_load_dwordx2 v[46:47], %[vp], %[rs], 0 offen offset:2048 sc1\n\t"
      "s_waitcnt vmcnt(9)\n\t"
      "v_mfma_f32_32x32x16_bf16 v[80:95], a[72:75], v[188:191], v[80:95]\n\t"
      "s_add_u32 %[tmp], %[sA], 12288\n\t"
      "buffer_load_dwordx4 v[224:227], %[v0], %[rs], %[tmp] offen offset:0 sc1\n\t"
      "v_mfma_f32_32x32x16_bf16 v[96:111], a[76:79], v[184:187], v[96:111]\n\t"
      "v_mfma_f32_32x32x16_bf16 a[240:255], a[76:79], v[188:191], a[240:255]\n\t"
      "buffer_load_dwordx4 v[228:231], %[v1], %[rs], %[tmp] offen offset:0 sc1\n\t"
      "v_mfma_f32_32x32x16_bf16 v[112:127], a[72:75], v[184:187], v[112:127]\n\t"
      "s_waitcnt vmcnt(9)\n\t"
      "v_mfma_f32_32x32x16_bf16 v[80:95], a[80:83], v[196:199], v[80:95]\n\t"
      "buffer_load_dwordx4 v[232:235], %[v0], %[rs], %[tmp] offen offset:1024 sc1\n\t"
      "v_mfma_f32_32x32x16_bf16 v[96:111], a[84:87], v[192:195], v[96:111]\n\t"
      "v_mfma_f32_32x32x16_bf16 a[240:255], a[84:87], v[196:199], a[240:255]\n\t"
      "buffer_load_dwordx4 v[236:239], %[v1], %[rs], %[tmp] offen offset:1024 sc1\n\t"
      "v_mfma_f32_32x32x16_bf16 v[112:127], a[80:83], v[192:195], v[112:127]\n\t"
      "s_waitcnt vmcnt(9)\n\t"
      "v_mfma_f32_32x32x16_bf16 v[80:95], a[88:91], v[204:207], v[80:95]\n\t"
      "buffer_load_dwordx4 v[240:243], %[v0], %[rs], %[tmp] offen offset:2048 sc1\n\t"
      "v_mfma_f32_32x32x16_bf16 v[96:111], a[92:95], v[200:203], v[96:111]\n\t"
      "v_mfma_f32_32x32x16_bf16 a[240:255], a[92:95], v[204:207], a[240:255]\n\t"
      "buffer_load_dwordx4 v[244:247], %[v1], %[rs], %[tmp] offen offset:2048 sc1\n\t"
      "v_mfma_f32_32x32x16_bf16 v[112:127], a[88:91], v[200:203], v[112:127]\n\t"
      "s_waitcnt vmcnt(9)\n\t"
      "v_mfma_f32_32x32x16_bf16 v[80:95], a[96:99], v[212:215], v[80:95]\n\t"
      "buffer_load_dwordx4 v[248:251], %[v0], %[rs], %[tmp] offen offset:3072 sc1\n\t"
      "v_mfma_f32_32x32x16_bf16 v[96:111], a[100:103], v[208:211], v[96:111]\n\t"
      "v_mfma_f32_32x32x16_bf16 a[240:255], a[100:103], v[212:215], a[240:255]\n\t"
      "buffer_load_dwordx4 v[252:255], %[v1], %[rs], %[tmp] offen offset:3072 sc1\n\t"
      "v_mfma_f32_32x32x16_bf16 v[112:127], a[96:99], v[208:211], v[112:127]\n\t"
      "s_waitcnt vmcnt(8)\n\t"
      "v_cmp_ne_u64 vcc, v[46:47], %[non]\n\t"
      "s_nop 3\n\t"
      "s_and_b32 vcc_lo, vcc_lo, 0xff\n\t"
      "s_cbranch_scc0 GBOK%=\n\t"
      "GBSPIN%=:\n\t"
      "buffer_load_dwordx2 v[46:47], %[vp], %[rs], 0 offen offset:2048 sc1\n\t"
      "s_add_u32 %[cnt], %[cnt], 1\n\t"
      "s_waitcnt vmcnt(0)\n\t"
      "v_cmp_ne_u64 vcc, v[46:47], %[non]\n\t"
      "s_nop 3\n\t"
      "s_and_b32 vcc_lo, vcc_lo, 0xff\n\t"
      "s_cbranch_scc0 GBOK%=\n\t"
      "s_cmp_lt_u32 %[cnt], 0x8000\n\t"
      "s_cbranch_scc1 GBSPIN%=\n\t"
      "GBOK%=:\n\t"
      "v_mfma_f32_32x32x16_bf16 v[80:95], a[104:107], v[220:223], v[80:95]\n\t"
      "s_add_u32 %[tmp], %[sB], 0\n\t"
      "buffer_load_dwordx4 v[128:131], %[v0], %[rs], %[tmp] offen offset:0 sc1\n\t"
      "v_mfma_f32_32x32x16_bf16 v[96:111], a[108:111], v[216:219], v[96:111]\n\t"
      "v_mfma_f32_32x32x16_bf16 a[240:255], a[108:111], v[220:223], a[240:255]\n\t"
      "buffer_load_dwordx4 v[132:135], %[v1], %[rs], %[tmp] offen offset:0 sc1\n\t"
      "v_mfma_f32_32x32x16_bf16 v[112:127], a[104:107], v[216:219], v[112:127]\n\t"
      "s_waitcnt vmcnt(8)\n\t"
      "v_mfma_f32_32x32x16_bf16 v[80:95], a[112:115], v[228:231], v[80:95]\n\t"
      "buffer_load_dwordx4 v[136:139], %[v0], %[rs], %[tmp] offen offset:1024 sc1\n\t"
      "v_mfma_f32_32x32x16_bf16 v[96:111], a[116:119], v[224:227], v[96:111]\n\t"
      "v_lshlrev_b32 v48, 16, v72\n\t"
      "v_and_b32 v49, 0xffff0000, v72\n\t"
      "v_lshlrev_b32 v50, 16, v73\n\t"
      "v_and_b32 v51, 0xffff0000, v73\n\t"
      "v_mfma_f32_32x32x16_bf16 a[240:255], a[116:119], v[228:231], a[240:255]\n\t"
      "buffer_load_dwordx4 v[140:143], %[v1], %[rs], %[tmp] offen offset:1024 sc1\n\t"
      "v_mfma_f32_32x32x16_bf16 v[112:127], a[112:115], v[224:227], v[112:127]\n\t"
      "v_lshlrev_b32 v52, 16, v74\n\t"
      "v_and_b32 v53, 0xffff0000, v74\n\t"
      "v_lshlrev_b32 v54, 16, v75\n\t"
      "v_and_b32 v55, 0xffff0000, v75\n\t"
      "s_waitcnt vmcnt(8)\n\t"
      "v_mfma_f32_32x32x16_bf16 v[80:95], a[120:123], v[236:239], v[80:95]\n\t"
      "buffer_load_dwordx4 v[144:147], %[v0], %[rs], %[tmp] offen offset:2048 sc1\n\t"
      "v_mfma_f32_32x32x16_bf16 v[96:111], a[124:127], v[232:235], v[96:111]\n\t"
      "v_lshlrev_b32 v56, 16, v76\n\t"
      "v_and_b32 v57, 0xffff0000, v76\n\t"
      "v_lshlrev_b32 v58, 16, v77\n\t"
      "v_and_b32 v59, 0xffff0000, v77\n\t"
      "v_mfma_f32_32x32x16_bf16 a[240:255], a[124:127], v[236:239], a[240:255]\n\t"
      "buffer_load_dwordx4 v[148:151], %[v1], %[rs], %[tmp] offen offset:2048 sc1\n\t"
      "v_mfma_f32_32x32x16_bf16 v[112:127], a[120:123], v[232:235], v[112:127]\n\t"
      "v_lshlrev_b32 v60, 16, v78\n\t"
      "v_and_b32 v61, 0xffff0000, v78\n\t"
      "v_lshlrev_b32 v62, 16, v79\n\t"
      "v_and_b32 v63, 0xffff0000, v79\n\t"
      "s_waitcnt vmcnt(8)\n\t"
      "v_mfma_f32_32x32x16_bf16 v[80:95], a[128:131], v[244:247], v[80:95]\n\t"
      "buffer_load_dwordx4 v[152:155], %[v0], %[rs], %[tmp] offen offset:3072 sc1\n\t"
      "v_mfma_f32_32x32x16_bf16 v[96:111], a[132:135], v[240:243], v[96:111]\n\t"
      "v_mfma_f32_32x32x16_bf16 a[240:255], a[132:135], v[244:247], a[240:255]\n\t"
      "buffer_load_dwordx4 v[156:159], %[v1], %[rs], %[tmp] offen offset:3072 sc1\n\t"
      "v_mfma_f32_32x32x16_bf16 v[112:127], a[128:131], v[240:243], v[112:127]\n\t"
      "s_waitcnt vmcnt(8)\n\t"
      "v_mfma_f32_32x32x16_bf16 v[80:95], a[136:139], v[252:255], v[80:95]\n\t"
      "s_add_u32 %[tmp], %[sB], 4096\n\t"
      "buffer_load_dwordx4 v[160:163], %[v0], %[rs], %[tmp] offen offset:0 sc1\n\t"
      "v_mfma_f32_32x32x16_bf16 v[96:111], a[140:143], v[248:251], v[96:111]\n\t"
      "v_mfma_f32_32x32x16_bf16 a[240:255], a[140:143], v[252:255], a[240:255]\n\t"
      "buffer_load_dwordx4 v[164:167], %[v1], %[rs], %[tmp] offen offset:0 sc1\n\t"
      "v_mfma_f32_32x32x16_bf16 v[112:127], a[136:139], v[248:251], v[112:127]\n\t"
      "s_waitcnt vmcnt(8)\n\t"
      "v_mfma_f32_32x32x16_bf16 v[80:95], a[144:147], v[132:135], v[80:95]\n\t"
      "buffer_load_dwordx4 v[168:171], %[v0], %[rs], %[tmp] offen offset:1024 sc1\n\t"
      "v_mfma_f32_32x32x16_bf16 v[96:111], a[148:151], v[128:131], v[96:111]\n\t"
      "v_mfma_f32_32x32x16_bf16 a[240:255], a[148:151], v[132:135], a[240:255]\n\t"
      "buffer_load_dwordx4 v[172:175], %[v1], %[rs], %[tmp] offen offset:1024 sc1\n\t"
      "v_mfma_f32_32x32x16_bf16 v[112:127], a[144:147], v[128:131], v[112:127]\n\t"
      "s_waitcnt vmcnt(8)\n\t"
      "v_mfma_f32_32x32x16_bf16 v[80:95], a[152:155], v[140:143], v[80:95]\n\t"
      "buffer_load_dwordx4 v[176:179], %[v0], %[rs], %[tmp] offen offset:2048 sc1\n\t"
      "v_mfma_f32_32x32x16_bf16 v[96:111], a[156:159], v[136:139], v[96:111]\n\t"
      "v_mfma_f32_32x32x16_bf16 a[240:255], a[156:159], v[140:143], a[240:255]\n\t"
      "buffer_load_dwordx4 v[180:183], %[v1], %[rs], %[tmp] offen offset:2048 sc1\n\t"
      "v_mfma_f32_32x32x16_bf16 v[112:127], a[152:155], v[136:139], v[112:127]\n\t"
      "s_waitcnt vmcnt(8)\n\t"
      "v_mfma_f32_32x32x16_bf16 v[80:95], a[160:163], v[148:151], v[80:95]\n\t"
      "buffer_load_dwordx4 v[184:187], %[v0], %[rs], %[tmp] offen offset:3072 sc1\n\t"
      "v_mfma_f32_32x32x16_bf16 v[96:111], a[164:167], v[144:147], v[96:111]\n\t"
      "v_mfma_f32_32x32x16_bf16 a[240:255], a[164:167], v[148:151], a[240:255]\n\t"
      "buffer_load_dwordx4 v[188:191], %[v1], %[rs], %[tmp] offen offset:3072 sc1\n\t"
      "v_mfma_f32_32x32x16_bf16 v[112:127], a[160:163], v[144:147], v[112:127]\n\t"
      "s_waitcnt vmcnt(8)\n\t"
      "v_mfma_f32_32x32x16_bf16 v[80:95], a[168:171], v[156:159], v[80:95]\n\t"
      "s_add_u32 %[tmp], %[sB], 8192\n\t"
      "buffer_load_dwordx4 v[192:195], %[v0], %[rs], %[tmp] offen offset:0 sc1\n\t"
      "v_mfma_f32_32x32x16_bf16 v[96:111], a[172:175], v[152:155], v[96:111]\n\t"
      "v_mfma_f32_32x32x16_bf16 a[240:255], a[172:175], v[156:159], a[240:255]\n\t"
      "buffer_load_dwordx4 v[196:199], %[v1], %[rs], %[tmp] offen offset:0 sc1\n\t"
      "v_mfma_f32_32x32x16_bf16 v[112:127], a[168:171], v[152:155], v[112:127]\n\t"
      "s_waitcnt vmcnt(8)\n\t"
      "v_mfma_f32_32x32x16_bf16 v[80:95], a[176:179], v[164:167], v[80:95]\n\t"
      "buffer_load_dwordx4 v[200:203], %[v0], %[rs], %[tmp] offen offset:1024 sc1\n\t"
      "v_mfma_f32_32x32x16_bf16 v[96:111], a[180:183], v[160:163], v[96:111]\n\t"
      "v_mfma_f32_32x32x16_bf16 a[240:255], a[180:183], v[164:167], a[240:255]\n\t"
      "buffer_load_dwordx4 v[204:207], %[v1], %[rs], %[tmp] offen offset:1024 sc1\n\t"
      "v_mfma_f32_32x32x16_bf16 v[112:127], a[176:179], v[160:163], v[112:127]\n\t"
      "s_waitcnt vmcnt(8)\n\t"
      "v_mfma_f32_32x32x16_bf16 v[80:95], a[184:187], v[172:175], v[80:95]\n\t"
      "buffer_load_dwordx4 v[208:211], %[v0], %[rs], %[tmp] offen offset:2048 sc1\n\t"
      "v_mfma_f32_32x32x16_bf16 v[96:111], a[188:191], v[168:171], v[96:111]\n\t"
      "v_mfma_f32_32x32x16_bf16 a[240:255], a[188:191], v[172:175], a[240:255]\n\t"
      "buffer_load_dwordx4 v[212:215], %[v1], %[rs], %[tmp] offen offset:2048 sc1\n\t"
      "v_mfma_f32_32x32x16_bf16 v[112:127], a[184:187], v[168:171], v[112:127]\n\t"
      "s_waitcnt vmcnt(8)\n\t"
      "v_mfma_f32_32x32x16_bf16 v[80:95], a[192:195], v[180:183], v[80:95]\n\t"
      "buffer_load_dwordx4 v[216:219], %[v0], %[rs], %[tmp] offen offset:3072 sc1\n\t"
      "v_mfma_f32_32x32x16_bf16 v[96:111], a[196:199], v[176:179], v[96:111]\n\t"
      "v_mfma_f32_32x32x16_bf16 a[240:255], a[196:199], v[180:183], a[240:255]\n\t"
      "buffer_load_dwordx4 v[220:223], %[v1], %[rs], %[tmp] offen offset:3072 sc1\n\t"
      "v_mfma_f32_32x32x16_bf16 v[112:127], a[192:195], v[176:179], v[112:127]\n\t"
      "s_waitcnt vmcnt(8)\n\t"
      "v_mfma_f32_32x32x16_bf16 v[80:95], a[200:203], v[188:191], v[80:95]\n\t"
      "v_mfma_f32_32x32x16_bf16 v[96:111], a[204:207], v[184:187], v[96:111]\n\t"
      "v_mfma_f32_32x32x16_bf16 a[240:255], a[204:207], v[188:191], a[240:255]\n\t"
      "v_mfma_f32_32x32x16_bf16 v[112:127], a[200:203], v[184:187], v[112:127]\n\t"
      "s_waitcnt vmcnt(6)\n\t"
      "v_mfma_f32_32x32x16_bf16 v[80:95], a[208:211], v[196:199], v[80:95]\n\t"
      "v_mfma_f32_32x32x16_bf16 v[96:111], a[212:215], v[192:195], v[96:111]\n\t"
      "v_mfma_f32_32x32x16_bf16 a[240:255], a[212:215], v[196:199], a[240:255]\n\t"
      "v_mfma_f32_32x32x16_bf16 v[112:127], a[208:211], v[192:195], v[112:127]\n\t"
      "s_waitcnt vmcnt(4)\n\t"
      "v_mfma_f32_32x32x16_bf16 v[80:95], a[216:219], v[204:207], v[80:95]\n\t"
      "v_mfma_f32_32x32x16_bf16 v[96:111], a[220:223], v[200:203], v[96:111]\n\t"
      "v_mfma_f32_32x32x16_bf16 a[240:255], a[220:223], v[204:207], a[240:255]\n\t"
      "v_mfma_f32_32x32x16_bf16 v[112:127], a[216:219], v[200:203], v[112:127]\n\t"
      "s_waitcnt vmcnt(2)\n\t"
      "v_mfma_f32_32x32x16_bf16 v[80:95], a[224:227], v[212:215], v[80:95]\n\t"
      "s_waitcnt vmcnt(0)\n\t"
      "v_mfma_f32_32x32x16_bf16 v[80:95], a[232:235], v[220:223], v[80:95]\n\t"
      "v_mfma_f32_32x32x16_bf16 v[96:111], a[228:231], v[208:211], v[96:111]\n\t"
      "s_nop 4\n\t"
      "v_mfma_f32_32x32x16_bf16 v[96:111], a[236:239], v[216:219], v[96:111]\n\t"
      "s_nop 4\n\t"
      "ds_write_b128 %[pW], v[80:83] offset:0\n\t"
      "v_mfma_f32_32x32x16_bf16 a[240:255], a[228:231], v[212:215], a[240:255]\n\t"
      "ds_write_b128 %[pW], v[84:87] offset:1024\n\t"
      "s_nop 4\n\t"
      "v_mfma_f32_32x32x16_bf16 a[240:255], a[236:239], v[220:223], a[240:255]\n\t"
      "ds_write_b128 %[pW], v[88:91] offset:2048\n\t"
      "s_nop 4\n\t"
      "ds_write_b128 %[pW], v[96:99] offset:4096\n\t"
      "v_mfma_f32_32x32x16_bf16 v[112:127], a[224:227], v[208:211], v[112:127]\n\t"
      "ds_write_b128 %[pW], v[92:95] offset:3072\n\t"
      "ds_write_b128 %[pW], v[100:103] offset:5120\n\t"
      "s_nop 4\n\t"
      "v_mfma_f32_32x32x16_bf16 v[112:127], a[232:235], v[216:219], v[112:127]\n\t"
      "ds_write_b128 %[pW], v[104:107] offset:6144\n\t"
      "ds_write_b128 %[pW], v[108:111] offset:7168\n\t"
      "s_nop 4\n\t"
      "ds_write_b128 %[pW], a[240:243] offset:8192\n\t"
      "ds_write_b128 %[pW], a[244:247] offset:9216\n\t"
      "ds_write_b128 %[pW], a[248:251] offset:10240\n\t"
      "ds_write_b128 %[pW], a[252:255] offset:11264\n\t"
      "s_waitcnt lgkmcnt(0)\n\t"
      "s_barrier\n\t"
      "ds_read_b32 v44, %[prr]\n\t"
      "ds_read_b128 v[128:131], %[p1] offset:0\n\t"
      "ds_read_b128 v[144:147], %[p2] offset:4096\n\t"
      "ds_read_b128 v[160:163], %[p3] offset:8192\n\t"
      "ds_read_b128 v[132:135], %[p1] offset:1024\n\t"
      "ds_read_b128 v[148:151], %[p2] offset:5120\n\t"
      "ds_read_b128 v[164:167], %[p3] offset:9216\n\t"
      "ds_read_b128 v[136:139], %[p1] offset:2048\n\t"
      "ds_read_b128 v[152:155], %[p2] offset:6144\n\t"
      "ds_read_b128 v[168:171], %[p3] offset:10240\n\t"
      "ds_read_b128 v[140:143], %[p1] offset:3072\n\t"
      "ds_read_b128 v[156:159], %[p2] offset:7168\n\t"
      "ds_read_b128 v[172:175], %[p3] offset:11264\n\t"
      "s_waitcnt lgkmcnt(9)\n\t"
      "v_pk_add_f32 v[112:113], v[112:113], v[128:129]\n\t"
      "v_pk_add_f32 v[144:145], v[144:145], v[160:161]\n\t"
      "v_pk_add_f32 v[114:115], v[114:115], v[130:131]\n\t"
      "v_pk_add_f32 v[146:147], v[146:147], v[162:163]\n\t"
      "v_pk_add_f32 v[112:113], v[112:113], v[144:145]\n\t"
      "v_pk_add_f32 v[114:115], v[114:115], v[146:147]\n\t"
      "v_pk_fma_f32 v[112:113], v[112:113], v[44:45], v[48:49] op_sel_hi:[1,0,1]\n\t"
      "v_pk_fma_f32 v[114:115], v[114:115], v[44:45], v[50:51] op_sel_hi:[1,0,1]\n\t"
      "v_cvt_pk_bf16_f32 v80, v112, v113\n\t"
      "v_cvt_pk_bf16_f32 v81, v114, v115\n\t"
      "s_waitcnt lgkmcnt(6)\n\t"
      "v_pk_add_f32 v[116:117], v[116:117], v[132:133]\n\t"
      "v_pk_add_f32 v[148:149], v[148:149], v[164:165]\n\t"
      "v_pk_add_f32 v[118:119], v[118:119], v[134:135]\n\t"
      "v_pk_add_f32 v[150:151], v[150:151], v[166:167]\n\t"
      "v_pk_add_f32 v[116:117], v[116:117], v[148:149]\n\t"
      "v_pk_add_f32 v[118:119], v[118:119], v[150:151]\n\t"
      "v_pk_fma_f32 v[116:117], v[116:117], v[44:45], v[52:53] op_sel_hi:[1,0,1]\n\t"
      "v_pk_fma_f32 v[118:119], v[118:119], v[44:45], v[54:55] op_sel_hi:[1,0,1]\n\t"
      "v_cvt_pk_bf16_f32 v82, v116, v117\n\t"
      "v_cvt_pk_bf16_f32 v83, v118, v119\n\t"
      "s_nop 0\n\t"
      "v_permlane32_swap_b32 v80, v82\n\t"
      "v_permlane32_swap_b32 v81, v83\n\t"
      "buffer_store_dwordx4 v[80:83], %[vo], %[ro], 0 offen offset:0\n\t"
      "s_waitcnt lgkmcnt(3)\n\t"
      "v_pk_add_f32 v[120:121], v[120:121], v[136:137]\n\t"
      "v_pk_add_f32 v[152:153], v[152:153], v[168:169]\n\t"
      "v_pk_add_f32 v[122:123], v[122:123], v[138:139]\n\t"
      "v_pk_add_f32 v[154:155], v[154:155], v[170:171]\n\t"
      "v_pk_add_f32 v[120:121], v[120:121], v[152:153]\n\t"
      "v_pk_add_f32 v[122:123], v[122:123], v[154:155]\n\t"
      "v_pk_fma_f32 v[120:121], v[120:121], v[44:45], v[56:57] op_sel_hi:[1,0,1]\n\t"
      "v_pk_fma_f32 v[122:123], v[122:123], v[44:45], v[58:59] op_sel_hi:[1,0,1]\n\t"
      "v_cvt_pk_bf16_f32 v84, v120, v121\n\t"
      "v_cvt_pk_bf16_f32 v85, v122, v123\n\t"
      "s_waitcnt lgkmcnt(0)\n\t"
      "v_pk_add_f32 v[124:125], v[124:125], v[140:141]\n\t"
      "v_pk_add_f32 v[156:157], v[156:157], v[172:173]\n\t"
      "v_pk_add_f32 v[126:127], v[126:127], v[142:143]\n\t"
      "v_pk_add_f32 v[158:159], v[158:159], v[174:175]\n\t"
      "v_pk_add_f32 v[124:125], v[124:125], v[156:157]\n\t"
      "v_pk_add_f32 v[126:127], v[126:127], v[158:159]\n\t"
      "v_pk_fma_f32 v[124:125], v[124:125], v[44:45], v[60:61] op_sel_hi:[1,0,1]\n\t"
      "v_pk_fma_f32 v[126:127], v[126:127], v[44:45], v[62:63] op_sel_hi:[1,0,1]\n\t"
      "v_cvt_pk_bf16_f32 v86, v124, v125\n\t"
      "v_cvt_pk_bf16_f32 v87, v126, v127\n\t"
      "s_nop 0\n\t"
      "v_permlane32_swap_b32 v84, v86\n\t"
      "v_permlane32_swap_b32 v85, v87\n\t"
      "buffer_store_dwordx4 v[84:87], %[vo], %[ro], 0 offen offset:32\n\t"
        : [tmp] "+s"(tmp), [cnt] "+s"(cnt)
        : [v0] "v"(hb + sh * (112 * 1024)), [v1] "v"(hb + (sh ^ 1) * (112 * 1024)), [rs] "s"(rws), [sA] "s"(w * 16384), [sB] "s"(65536 + w * 12288),
          [pW] "v"(stA + lane * 16), [hf] "v"(hf + (64 + 3 * mem) * 1024), [vf] "v"(vf), [vp] "v"(vp), [non] "v"(non), [prs] "v"(ring + 12160 + (lane & 31) * 4),
          [prr] "v"(L_RING + (2 * th + sh) * 12288 + 12160 + (lane & 31) * 4),
          [p1] "v"(L_W1A + (w ^ 1) * 16384 + lane * 16), [p2] "v"(L_W1A + (w ^ 2) * 16384 + lane * 16), [p3] "v"(L_W1A + (w ^ 3) * 16384 + lane * 16),
          [ro] "s"(ro),
          [vo] "v"(((128 * grp + 64 * th + 32 * sh + (lane & 31)) * 512 + 64 * ds + 32 * cw + 8 * (lane >> 5)) * 2)
        : "memory", "scc", "vcc");
  }
}
}

extern "C" void kernel_launch(void* const* d_in, const int* in_sizes, int n_in, void* d_out, int out_size, void* d_ws, size_t ws_size,
                              hipStream_t stream) {
  using namespace s4k;
  (void)in_sizes; (void)n_in; (void)out_size; (void)ws_size;
  (void)hipFuncSetAttribute((const void*)s4_mlp_kernel, hipFuncAttributeMaxDynamicSharedMemorySize, LDS_BYTES);
  s4_mlp_kernel<<<256, 256, LDS_BYTES, stream>>>((const u16*)d_in[0], (const u16*)d_in[1], (const u16*)d_in[2], (const u16*)d_in[3],
                                                 (const u16*)d_in[4], (const u16*)d_in[5], (const u16*)d_in[6], (const u16*)d_in[7],
                                                 (const u16*)d_in[8], (const u16*)d_in[10], (const u16*)d_in[11], (u16*)d_out,
                                                 (unsigned char*)d_ws);
}
```
